```python
import torch

def _softmax(x, axis=-1):
    return torch.softmax(x, dim=axis)
def _gelu(x):
    return torch.nn.functional.gelu(x, approximate="tanh")
def _silu(x):
    return torch.nn.functional.silu(x)
def _sigmoid(x):
    return torch.sigmoid(x)
def _layernorm(x, axis=-1, eps=1e-5):
    m = x.mean(dim=axis, keepdim=True)
    v = x.var(dim=axis, keepdim=True, unbiased=False)
    return (x - m) / torch.sqrt(v + eps)
def _scatter_add(tgt, idx, src, axis=0):
    return tgt.clone().index_add_(axis, idx, src)
def _segment_sum(src, seg, n):
    out = torch.zeros((n,) + src.shape[1:], dtype=src.dtype)
    return out.index_add_(0, seg, src)
def _pad(x, pw):
    flat = tuple(v for pair in reversed(pw) for v in pair)
    return torch.nn.functional.pad(x, flat)

kernel_name = 'standalone__glu_gated_bilinear__prompt'

def setup_inputs():

    _w = (lambda a: (lambda t: t.to(torch.bfloat16) if t.is_floating_point() else t)(torch.as_tensor(a)))
    B, S, D, F = 1, 2048, 512, 1792
    return {
        "x": _w((lambda *s: torch.randn(*s, dtype=torch.bfloat16) * 0.05)(B, S, D)),
        "w_gate": _w((lambda *s: torch.randn(*s, dtype=torch.bfloat16) * 0.05)(D, F) * (D ** -0.5)),
        "w_up": _w((lambda *s: torch.randn(*s, dtype=torch.bfloat16) * 0.05)(D, F) * (D ** -0.5)),
        "w_down": _w((lambda *s: torch.randn(*s, dtype=torch.bfloat16) * 0.05)(F, D) * (F ** -0.5)),
    }

def reference(x, w_gate, w_up, w_down):
    g = torch.einsum("bsd,df->bsf", x, w_gate)
    u = torch.einsum("bsd,df->bsf", x, w_up)
    h = g * u
    return torch.einsum("bsf,fd->bsd", h, w_down)
```

```cpp
#include <hip/hip_runtime.h>
#include <hip/hip_bf16.h>
typedef __attribute__((ext_vector_type(8))) __bf16 bf16x8;
typedef __attribute__((ext_vector_type(4))) float f32x4;
constexpr int M=2048, D=512, F=1792;

__global__ __launch_bounds__(256) void k1(const __bf16* __restrict__ x, const __bf16* __restrict__ wg, const __bf16* __restrict__ wu, __bf16* __restrict__ h){
  __shared__ __bf16 sg[64][40], su[64][40];
  int lane=threadIdx.x&63, w=threadIdx.x>>6;
  int m0=blockIdx.y*64, f0=blockIdx.x*64;
  f32x4 ag[4]={}, au[4]={};
  for(int k0=0;k0<D;k0+=32){
    __syncthreads();
    for(int i=threadIdx.x;i<32*64;i+=256){int k=i>>6,f=i&63; sg[f][k]=wg[(k0+k)*F+f0+f]; su[f][k]=wu[(k0+k)*F+f0+f];}
    __syncthreads();
    bf16x8 a=*(const bf16x8*)(x+(m0+w*16+(lane&15))*D+k0+(lane>>4)*8);
    for(int n=0;n<4;n++){
      bf16x8 bg=*(const bf16x8*)&sg[n*16+(lane&15)][(lane>>4)*8];
      bf16x8 bu=*(const bf16x8*)&su[n*16+(lane&15)][(lane>>4)*8];
      ag[n]=__builtin_amdgcn_mfma_f32_16x16x32_bf16(a,bg,ag[n],0,0,0);
      au[n]=__builtin_amdgcn_mfma_f32_16x16x32_bf16(a,bu,au[n],0,0,0);
    }
  }
  for(int n=0;n<4;n++)for(int j=0;j<4;j++) h[(m0+w*16+(lane>>4)*4+j)*F+f0+n*16+(lane&15)]=(__bf16)(ag[n][j]*au[n][j]);
}
__global__ __launch_bounds__(256) void k2(const __bf16* __restrict__ h, const __bf16* __restrict__ wd, __bf16* __restrict__ out){
  __shared__ __bf16 sd[64][40];
  int lane=threadIdx.x&63, w=threadIdx.x>>6;
  int m0=blockIdx.y*64, d0=blockIdx.x*64;
  f32x4 acc[4]={};
  for(int k0=0;k0<F;k0+=32){
    __syncthreads();
    for(int i=threadIdx.x;i<32*64;i+=256){int k=i>>6,f=i&63; sd[f][k]=wd[(k0+k)*D+d0+f];}
    __syncthreads();
    bf16x8 a=*(const bf16x8*)(h+(m0+w*16+(lane&15))*F+k0+(lane>>4)*8);
    for(int n=0;n<4;n++){
      bf16x8 b=*(const bf16x8*)&sd[n*16+(lane&15)][(lane>>4)*8];
      acc[n]=__builtin_amdgcn_mfma_f32_16x16x32_bf16(a,b,acc[n],0,0,0);
    }
  }
  for(int n=0;n<4;n++)for(int j=0;j<4;j++) out[(m0+w*16+(lane>>4)*4+j)*D+d0+n*16+(lane&15)]=(__bf16)(acc[n][j]);
}
extern "C" void kernel_launch(void* const* d_in, const int* in_sizes, int n_in, void* d_out, int out_size, void* d_ws, size_t ws_size, hipStream_t stream) {
  const __bf16* x=(const __bf16*)d_in[0]; const __bf16* wg=(const __bf16*)d_in[1]; const __bf16* wu=(const __bf16*)d_in[2]; const __bf16* wd=(const __bf16*)d_in[3];
  __bf16* h=(__bf16*)d_ws;
  k1<<<dim3(F/64,M/64),256,0,stream>>>(x,wg,wu,h);
  k2<<<dim3(D/64,M/64),256,0,stream>>>(h,wd,(__bf16*)d_out);
}
```

```cpp
#include <hip/hip_runtime.h>
#include <hip/hip_bf16.h>
typedef __attribute__((ext_vector_type(8))) __bf16 bf16x8;
typedef __attribute__((ext_vector_type(4))) float f32x4;
constexpr int M=2048, D=512, F=1792;

__global__ __launch_bounds__(256) void k1(const __bf16* __restrict__ x, const __bf16* __restrict__ wg, const __bf16* __restrict__ wu, __bf16* __restrict__ h){
  __shared__ __bf16 sg[64][40], su[64][40];
  int lane=threadIdx.x&63, w=threadIdx.x>>6;
  int m0=blockIdx.y*64, f0=blockIdx.x*64;
  f32x4 ag[4]={}, au[4]={};
  for(int k0=0;k0<D;k0+=32){
    __syncthreads();
    for(int i=threadIdx.x;i<32*64;i+=256){int k=i>>6,f=i&63; sg[f][k]=wg[(k0+k)*F+f0+f]; su[f][k]=wu[(k0+k)*F+f0+f];}
    __syncthreads();
    bf16x8 a=*(const bf16x8*)(x+(m0+w*16+(lane&15))*D+k0+(lane>>4)*8);
    for(int n=0;n<4;n++){
      bf16x8 bg=*(const bf16x8*)&sg[n*16+(lane&15)][(lane>>4)*8];
      bf16x8 bu=*(const bf16x8*)&su[n*16+(lane&15)][(lane>>4)*8];
      ag[n]=__builtin_amdgcn_mfma_f32_16x16x32_bf16(a,bg,ag[n],0,0,0);
      au[n]=__builtin_amdgcn_mfma_f32_16x16x32_bf16(a,bu,au[n],0,0,0);
    }
  }
  for(int n=0;n<4;n++)for(int j=0;j<4;j++) h[(m0+w*16+(lane>>4)*4+j)*F+f0+n*16+(lane&15)]=(__bf16)(ag[n][j]*au[n][j]);
}
__global__ __launch_bounds__(256) void k2(const __bf16* __restrict__ h, const __bf16* __restrict__ wd, __bf16* __restrict__ out){
  __shared__ __bf16 sd[64][40];
  int lane=threadIdx.x&63, w=threadIdx.x>>6;
  int m0=blockIdx.y*64, d0=blockIdx.x*64;
  f32x4 acc[4]={};
  for(int k0=0;k0<F;k0+=32){
    __syncthreads();
    for(int i=threadIdx.x;i<32*64;i+=256){int k=i>>6,f=i&63; sd[f][k]=wd[(k0+k)*D+d0+f];}
    __syncthreads();
    bf16x8 a=*(const bf16x8*)(h+(m0+w*16+(lane&15))*F+k0+(lane>>4)*8);
    for(int n=0;n<4;n++){
      bf16x8 b=*(const bf16x8*)&sd[n*16+(lane&15)][(lane>>4)*8];
      acc[n]=__builtin_amdgcn_mfma_f32_16x16x32_bf16(a,b,acc[n],0,0,0);
    }
  }
  for(int n=0;n<4;n++)for(int j=0;j<4;j++) out[(m0+w*16+(lane>>4)*4+j)*D+d0+n*16+(lane&15)]=(__bf16)(acc[n][j]);
}
typedef __attribute__((address_space(3))) void* lds_ptr_t;
#define WAITV(n) asm volatile("s_waitcnt vmcnt(" #n ")" ::: "memory")
template<int D> __device__ __forceinline__ void waitv(){
  if (D==4) WAITV(4); else if (D==8) WAITV(8); else if (D==12) WAITV(12); else if (D==16) WAITV(16); else if (D==24) WAITV(24); else if (D==28) WAITV(28); else if (D==32) WAITV(32); else WAITV(0);
}
__device__ __forceinline__ unsigned long long now(){ unsigned long long t; asm volatile("s_memtime %0\n\ts_waitcnt lgkmcnt(0)" : "=s"(t) :: "memory"); return t; }
__device__ __forceinline__ unsigned long long rnow(){ unsigned long long t; asm volatile("s_memrealtime %0\n\ts_waitcnt lgkmcnt(0)" : "=s"(t) :: "memory"); return t; }

template<int DEPTH, int MAP1, int MAP2, int AUXW>
__global__ __launch_bounds__(256) void skel(const char* __restrict__ x, const char* __restrict__ wg, const char* __restrict__ wu, const char* __restrict__ wd, const char* __restrict__ h, unsigned long long* out){
  extern __shared__ __attribute__((aligned(16))) char smem[];
  const int lane = threadIdx.x & 63;
  const int wave = __builtin_amdgcn_readfirstlane(threadIdx.x >> 6);
  auto rx = __builtin_amdgcn_make_buffer_rsrc((void*)x, 0, 2048*512*2, 0x00020000);
  auto rg = __builtin_amdgcn_make_buffer_rsrc((void*)wg, 0, 1792*512*2, 0x00020000);
  auto ru = __builtin_amdgcn_make_buffer_rsrc((void*)wu, 0, 1792*512*2, 0x00020000);
  auto rd = __builtin_amdgcn_make_buffer_rsrc((void*)wd, 0, 1792*512*2, 0x00020000);
  auto rh = __builtin_amdgcn_make_buffer_rsrc((void*)h, 0, 2048*1792*2, 0x00020000);
  const int b = blockIdx.x, xcd = b & 7, r = b >> 3;
  unsigned long long rt0 = rnow();
  unsigned long long t0 = now(), t1 = t0, t2 = t0, t3, t4;
  int slot = 0;
#define ldsp(s) ((lds_ptr_t)(smem + (wave * 32 + ((s) & 31)) * 1024))
  int mt, fs; bool active;
  if (MAP1 == 0) { active = r < 28; mt = (xcd >> 1) * 4 + (r & 3); fs = (xcd & 1) * 7 + (r >> 2); }
  else if (MAP1 == 1) { active = r < 28; mt = xcd * 2 + (r & 1); fs = r >> 1; }
  else { active = b < 224; mt = b & 15; fs = b >> 4; }
  const unsigned row8 = lane >> 3, c8 = lane & 7;
  if (active) {
    unsigned vx = (mt * 128 + wave * 32 + row8) * 1024 + c8 * 16;
    unsigned vw = (wave * 16 + row8) * 3584 + fs * 256 + c8 * 16;
    for (int kt = 0; kt < 8; ++kt) {
#pragma unroll
      for (int j = 0; j < 4; ++j) __builtin_amdgcn_raw_ptr_buffer_load_lds(rx, ldsp(slot + j), 16, vx, kt * 128 + j * 8 * 1024, 0, 0);
#pragma unroll
      for (int j = 0; j < 2; ++j) __builtin_amdgcn_raw_ptr_buffer_load_lds(rg, ldsp(slot + 4 + j), 16, vw, (kt * 64 + j * 8) * 3584, 0, AUXW);
#pragma unroll
      for (int j = 0; j < 2; ++j) __builtin_amdgcn_raw_ptr_buffer_load_lds(ru, ldsp(slot + 6 + j), 16, vw, (kt * 64 + j * 8) * 3584, 0, AUXW);
      slot += 8;
      waitv<DEPTH>();
    }
    t1 = now();
    for (int kt = 0; kt < 8; ++kt) {
#pragma unroll
      for (int j = 0; j < 2; ++j) __builtin_amdgcn_raw_ptr_buffer_load_lds(rg, ldsp(slot + j), 16, vw, (kt * 64 + j * 8) * 3584 + 128, 0, AUXW);
#pragma unroll
      for (int j = 0; j < 2; ++j) __builtin_amdgcn_raw_ptr_buffer_load_lds(ru, ldsp(slot + 2 + j), 16, vw, (kt * 64 + j * 8) * 3584 + 128, 0, AUXW);
      slot += 4;
      waitv<DEPTH>();
    }
    t2 = now();
  }
  int m2, d2;
  if (MAP2 == 0) { m2 = xcd * 4 + (r & 3); d2 = r >> 2; }
  else if (MAP2 == 1) { m2 = (xcd >> 1) * 8 + (r & 7); d2 = (xcd & 1) * 4 + (r >> 3); }
  else { m2 = b >> 3; d2 = b & 7; }
  {
    unsigned vd = (wave * 448 + row8) * 1024 + d2 * 128 + c8 * 16;
    for (int i = 0; i < 14; ++i) {
#pragma unroll
      for (int j = 0; j < 4; ++j) __builtin_amdgcn_raw_ptr_buffer_load_lds(rd, ldsp(slot + j), 16, vd, (i * 32 + j * 8) * 1024, 0, AUXW);
      slot += 4;
      waitv<DEPTH>();
    }
    t3 = now();
    unsigned vh = m2 * 229376 + wave * 57344 + lane * 16;
    for (int i = 0; i < 14; ++i) {
#pragma unroll
      for (int j = 0; j < 4; ++j) __builtin_amdgcn_raw_ptr_buffer_load_lds(rh, ldsp(slot + j), 16, vh, (i * 4 + j) * 1024, 0, 16);
      slot += 4;
      waitv<DEPTH>();
    }
  }
  WAITV(0);
  __syncthreads();
  t4 = now();
  unsigned long long rt1 = rnow();
  if (threadIdx.x == 0) { out[b*8+0] = t1 - t0; out[b*8+1] = t2 - t0; out[b*8+2] = t3 - t0; out[b*8+3] = t4 - t0; out[b*8+4] = rt0; out[b*8+5] = rt1; out[b*8+6] = active; }
}

__global__ void spin(const unsigned long long* o){
  if (threadIdx.x==0){
    unsigned long long mn=~0ull,mx=0,mxs=0,mne=~0ull; unsigned long long s0=0,s1=0,s2=0,s3=0; int n=0;
    for(int b=0;b<256;++b){ unsigned long long st=o[b*8+4], en=o[b*8+5]; mn=st<mn?st:mn; mx=en>mx?en:mx; mxs=st>mxs?st:mxs; mne=en<mne?en:mne;
      if(o[b*8+6]){ s0+=o[b*8]; s1+=o[b*8+1]; s2+=o[b*8+2]; s3+=o[b*8+3]; n++; } }
    unsigned long long metric = (mxs-mn);
    unsigned long long t0=rnow();
    unsigned long long lim = metric*100;
    if (lim > 2000000ull) lim = 2000000ull;
    while(rnow()-t0 < lim) __builtin_amdgcn_s_sleep(1);
  }
}
extern "C" void kernel_launch(void* const* d_in, const int* in_sizes, int n_in, void* d_out, int out_size, void* d_ws, size_t ws_size, hipStream_t stream) {
  const __bf16* x=(const __bf16*)d_in[0]; const __bf16* wg=(const __bf16*)d_in[1]; const __bf16* wu=(const __bf16*)d_in[2]; const __bf16* wd=(const __bf16*)d_in[3];
  __bf16* h=(__bf16*)d_ws;
  unsigned long long* o=(unsigned long long*)((char*)d_ws+(16<<20));
  auto kf = skel<24,0,0,0>;
  hipFuncSetAttribute((const void*)kf, hipFuncAttributeMaxDynamicSharedMemorySize, 128*1024);
  kf<<<256,256,128*1024,stream>>>((const char*)x,(const char*)wg,(const char*)wu,(const char*)wd,(const char*)h,o);
  k1<<<dim3(F/64,M/64),256,0,stream>>>(x,wg,wu,h);
  k2<<<dim3(D/64,M/64),256,0,stream>>>(h,wd,(__bf16*)d_out);
  spin<<<1,64,0,stream>>>(o);
}
```

```cpp
#include <hip/hip_runtime.h>
#include <hip/hip_bf16.h>
typedef __attribute__((ext_vector_type(8))) __bf16 bf16x8;
typedef __attribute__((ext_vector_type(4))) float f32x4;
constexpr int M=2048, D=512, F=1792;

__global__ __launch_bounds__(256) void k1(const __bf16* __restrict__ x, const __bf16* __restrict__ wg, const __bf16* __restrict__ wu, __bf16* __restrict__ h){
  __shared__ __bf16 sg[64][40], su[64][40];
  int lane=threadIdx.x&63, w=threadIdx.x>>6;
  int m0=blockIdx.y*64, f0=blockIdx.x*64;
  f32x4 ag[4]={}, au[4]={};
  for(int k0=0;k0<D;k0+=32){
    __syncthreads();
    for(int i=threadIdx.x;i<32*64;i+=256){int k=i>>6,f=i&63; sg[f][k]=wg[(k0+k)*F+f0+f]; su[f][k]=wu[(k0+k)*F+f0+f];}
    __syncthreads();
    bf16x8 a=*(const bf16x8*)(x+(m0+w*16+(lane&15))*D+k0+(lane>>4)*8);
    for(int n=0;n<4;n++){
      bf16x8 bg=*(const bf16x8*)&sg[n*16+(lane&15)][(lane>>4)*8];
      bf16x8 bu=*(const bf16x8*)&su[n*16+(lane&15)][(lane>>4)*8];
      ag[n]=__builtin_amdgcn_mfma_f32_16x16x32_bf16(a,bg,ag[n],0,0,0);
      au[n]=__builtin_amdgcn_mfma_f32_16x16x32_bf16(a,bu,au[n],0,0,0);
    }
  }
  for(int n=0;n<4;n++)for(int j=0;j<4;j++) h[(m0+w*16+(lane>>4)*4+j)*F+f0+n*16+(lane&15)]=(__bf16)(ag[n][j]*au[n][j]);
}
__global__ __launch_bounds__(256) void k2(const __bf16* __restrict__ h, const __bf16* __restrict__ wd, __bf16* __restrict__ out){
  __shared__ __bf16 sd[64][40];
  int lane=threadIdx.x&63, w=threadIdx.x>>6;
  int m0=blockIdx.y*64, d0=blockIdx.x*64;
  f32x4 acc[4]={};
  for(int k0=0;k0<F;k0+=32){
    __syncthreads();
    for(int i=threadIdx.x;i<32*64;i+=256){int k=i>>6,f=i&63; sd[f][k]=wd[(k0+k)*D+d0+f];}
    __syncthreads();
    bf16x8 a=*(const bf16x8*)(h+(m0+w*16+(lane&15))*F+k0+(lane>>4)*8);
    for(int n=0;n<4;n++){
      bf16x8 b=*(const bf16x8*)&sd[n*16+(lane&15)][(lane>>4)*8];
      acc[n]=__builtin_amdgcn_mfma_f32_16x16x32_bf16(a,b,acc[n],0,0,0);
    }
  }
  for(int n=0;n<4;n++)for(int j=0;j<4;j++) out[(m0+w*16+(lane>>4)*4+j)*D+d0+n*16+(lane&15)]=(__bf16)(acc[n][j]);
}

__device__ __forceinline__ unsigned long long now(){ unsigned long long t; asm volatile("s_memtime %0\n\ts_waitcnt lgkmcnt(0)" : "=s"(t) :: "memory"); return t; }
__device__ __forceinline__ unsigned long long rnow(){ unsigned long long t; asm volatile("s_memrealtime %0\n\ts_waitcnt lgkmcnt(0)" : "=s"(t) :: "memory"); return t; }
__global__ __launch_bounds__(512) void ka(unsigned long long* o_unused, const char* x_unused){
  extern __shared__ char smem[];
  unsigned long long r0 = rnow();
  unsigned long long t0 = now();
  unsigned long long op, xp;
  auto kp = __builtin_amdgcn_kernarg_segment_ptr();
  asm volatile("s_load_dwordx2 %0, %2, 0x0\n\ts_load_dwordx2 %1, %2, 0x8\n\ts_waitcnt lgkmcnt(0)" : "=&s"(op), "=&s"(xp) : "s"(kp) : "memory");
  unsigned long long t1 = now();
  unsigned v;
  const char* p = (const char*)xp + blockIdx.x * 8192 + (threadIdx.x & 63) * 128;
  asm volatile("global_load_dword %0, %1, off\n\ts_waitcnt vmcnt(0)" : "=v"(v) : "v"(p) : "memory");
  unsigned long long t2 = now();
  unsigned long long r1 = rnow();
  if (threadIdx.x == 0) { unsigned long long* o = (unsigned long long*)op; o[blockIdx.x*8+0] = t1 - t0; o[blockIdx.x*8+1] = t2 - t1; o[blockIdx.x*8+2] = v; o[blockIdx.x*8+4] = r0; o[blockIdx.x*8+5] = r1; }
}

__global__ void spin(const unsigned long long* o){
  if (threadIdx.x==0){
    unsigned long long mn=~0ull,mx=0,mxs=0,mne=~0ull; unsigned long long s0=0,s1=0,m0=0,m1=0; int n=0;
    for(int b=0;b<256;++b){ unsigned long long st=o[b*8+4], en=o[b*8+5]; mn=st<mn?st:mn; mx=en>mx?en:mx; mxs=st>mxs?st:mxs; mne=en<mne?en:mne;
      s0+=o[b*8]; s1+=o[b*8+1]; m0=o[b*8]>m0?o[b*8]:m0; m1=o[b*8+1]>m1?o[b*8+1]:m1; n++; }
    unsigned long long metric = 0;
    unsigned long long t0=rnow();
    unsigned long long lim = metric*100;
    if (lim > 2000000ull) lim = 2000000ull;
    while(rnow()-t0 < lim) __builtin_amdgcn_s_sleep(1);
  }
}
extern "C" void kernel_launch(void* const* d_in, const int* in_sizes, int n_in, void* d_out, int out_size, void* d_ws, size_t ws_size, hipStream_t stream) {
  const __bf16* x=(const __bf16*)d_in[0]; const __bf16* wg=(const __bf16*)d_in[1]; const __bf16* wu=(const __bf16*)d_in[2]; const __bf16* wd=(const __bf16*)d_in[3];
  __bf16* h=(__bf16*)d_ws;
  unsigned long long* o=(unsigned long long*)((char*)d_ws+(16<<20));
  hipFuncSetAttribute((const void*)ka, hipFuncAttributeMaxDynamicSharedMemorySize, 160*1024);
  ka<<<256,512,160*1024,stream>>>(o,(const char*)x);
  k1<<<dim3(F/64,M/64),256,0,stream>>>(x,wg,wu,h);
  k2<<<dim3(D/64,M/64),256,0,stream>>>(h,wd,(__bf16*)d_out);
  spin<<<1,64,0,stream>>>(o);
}
```

```cpp
#include <hip/hip_runtime.h>
#include <hip/hip_bf16.h>

__global__ __launch_bounds__(256, 1) void glu_fused(const void* x, const void* wg, const void* wu, const void* wd, void* out, void* ws) {
  __shared__ __attribute__((aligned(1024))) char smem[163840];
  unsigned tid = threadIdx.x;
  asm volatile(
    ".Lg_entry:\n"
    "s_load_dwordx8 s[8:15], %1, 0x0\n"
    "s_load_dwordx4 s[16:19], %1, 0x20\n"
    "s_mov_b32 s48, %2\n"
    "v_lshrrev_b32 v1, 6, %0\n"
    "v_and_b32 v232, 63, %0\n"
    "v_readfirstlane_b32 s51, v1\n"
    "s_cmp_ge_u32 s48, 8\n"
    "s_cbranch_scc1 .Lg_nopf\n"
    "s_getpc_b64 s[4:5]\n"
    ".Lg_pc:\n"
    "v_lshlrev_b32 v6, 7, %0\n"
    "v_min_u32 v6, .Lg_last-.Lg_pc, v6\n"
    "global_load_dword v136, v6, s[4:5]\n"
    ".Lg_nopf:\n"
    "s_and_b32 s49, s48, 7\n"
    "s_lshr_b32 s50, s48, 3\n"
    "s_lshr_b32 s57, s49, 1\n"
    "s_and_b32 s58, s49, 1\n"
    "s_and_b32 s59, s50, 3\n"
    "s_lshr_b32 s60, s50, 2\n"
    "s_lshl2_add_u32 s52, s57, s59\n"
    "s_mul_i32 s53, s58, 7\n"
    "s_add_u32 s53, s53, s60\n"
    "s_sub_u32 s53, s53, 1\n"
    "s_lshl1_add_u32 s56, s58, s57\n"
    "s_lshl_b32 s56, s56, 1\n"
    "s_add_u32 s62, s59, s60\n"
    "s_and_b32 s62, s62, 1\n"
    "s_add_u32 s56, s56, s62\n"
    "v_lshrrev_b32 v4, 3, v232\n"
    "v_and_b32 v5, 7, v232\n"
    "v_lshrrev_b32 v6, 4, v232\n"
    "v_xor_b32 v7, v5, v6\n"
    "s_lshl_b32 s61, s51, 15\n"
    "v_lshl_add_u32 v8, v4, 10, s61\n"
    "v_lshl_add_u32 v192, v7, 4, v8\n"
    "v_xor_b32 v193, 64, v192\n"
    "v_add_u32 v194, 14336, v192\n"
    "v_add_u32 v195, 21504, v193\n"
    "v_add_u32 v193, 7168, v193\n"
    "v_and_b32 v7, 1, v6\n"
    "v_lshlrev_b32 v7, 1, v7\n"
    "v_xor_b32 v7, v5, v7\n"
    "s_lshl_b32 s61, s51, 4\n"
    "v_add_u32 v8, s61, v4\n"
    "v_mul_u32_u24 v8, 3584, v8\n"
    "v_lshl_add_u32 v196, v7, 4, v8\n"
    "v_xor_b32 v197, 64, v196\n"
    "v_add_u32 v197, 27648, v197\n"
    "s_lshl_b32 s63, s51, 12\n"
    "s_lshl_b32 s61, s52, 17\n"
    "s_lshl_b32 s62, s53, 8\n"
    "s_add_u32 s6, s56, 0\n"
    "s_and_b32 s6, s6, 7\n"
    "s_lshl_b32 s64, s6, 7\n"
    "s_add_u32 s64, s64, s61\n"
    "s_mul_i32 s72, s6, 229376\n"
    "s_add_u32 s72, s72, s62\n"
    "s_mov_b32 s22, 2097152\n"
    "s_mov_b32 s23, 0x20000\n"
    "s_mov_b32 s26, 1835008\n"
    "s_mov_b32 s27, 0x20000\n"
    "s_mov_b32 s30, 1837056\n"
    "s_mov_b32 s31, 0x20000\n"
    "s_waitcnt lgkmcnt(0)\n"
    "s_mov_b32 s20, s8\n"
    "s_and_b32 s21, s9, 0xffff\n"
    "s_mov_b32 s24, s10\n"
    "s_and_b32 s25, s11, 0xffff\n"
    "s_sub_u32 s28, s12, 2048\n"
    "s_subb_u32 s29, s13, 0\n"
    "s_and_b32 s29, s29, 0xffff\n"
    "s_mov_b32 s32, s14\n"
    "s_and_b32 s33, s15, 0xffff\n"
    "s_add_u32 s4, s18, 10518528\n"
    "s_addc_u32 s5, s19, 0\n"
    "s_lshl_b32 s15, s57, 7\n"
    "s_load_dwordx2 s[46:47], s[4:5], s15\n"
    "s_cmp_lt_u32 s50, 4\n"
    "s_cbranch_scc0 .Lg_main\n"
    "s_mov_b32 s34, 1835008\n"
    "s_mov_b32 s35, 0x20000\n"
    "s_lshl2_add_u32 s6, s48, s51\n"
    "s_mul_i32 s6, s6, 7\n"
    "v_min_u32 v234, 6, v4\n"
    "v_add_u32 v234, s6, v234\n"
    "v_and_b32 v235, 0x3c0, v234\n"
    "v_add_u32 v234, v234, v235\n"
    "v_lshlrev_b32 v234, 10, v234\n"
    "v_lshl_add_u32 v234, v5, 7, v234\n"
    "s_mov_b32 s6, 0\n"
    "buffer_load_dword v137, v234, s[32:35], s6 offen\n"
    "s_branch .Lg_spare\n"
    ".Lg_main:\n"
    "s_add_u32 m0, s63, 0\n"
    "s_nop 0\n"
    "buffer_load_dwordx4 v192, s[20:23], s64 offen offset:0 lds\n"
    "s_add_u32 s6, s56, 1\n"
    "s_and_b32 s6, s6, 7\n"
    "s_lshl_b32 s65, s6, 7\n"
    "s_add_u32 s65, s65, s61\n"
    "s_mul_i32 s73, s6, 229376\n"
    "s_add_u32 s73, s73, s62\n"
    "s_add_u32 s6, s56, 2\n"
    "s_and_b32 s6, s6, 7\n"
    "s_lshl_b32 s66, s6, 7\n"
    "s_add_u32 s66, s66, s61\n"
    "s_mul_i32 s74, s6, 229376\n"
    "s_add_u32 s74, s74, s62\n"
    "buffer_load_dwordx4 v193, s[20:23], s64 offen offset:1024 lds\n"
    "s_add_u32 s6, s56, 3\n"
    "s_and_b32 s6, s6, 7\n"
    "s_lshl_b32 s67, s6, 7\n"
    "s_add_u32 s67, s67, s61\n"
    "s_mul_i32 s75, s6, 229376\n"
    "s_add_u32 s75, s75, s62\n"
    "s_add_u32 s6, s56, 4\n"
    "s_and_b32 s6, s6, 7\n"
    "s_lshl_b32 s68, s6, 7\n"
    "s_add_u32 s68, s68, s61\n"
    "s_mul_i32 s76, s6, 229376\n"
    "s_add_u32 s76, s76, s62\n"
    "buffer_load_dwordx4 v194, s[20:23], s64 offen offset:2048 lds\n"
    "s_add_u32 s6, s56, 5\n"
    "s_and_b32 s6, s6, 7\n"
    "s_lshl_b32 s69, s6, 7\n"
    "s_add_u32 s69, s69, s61\n"
    "s_mul_i32 s77, s6, 229376\n"
    "s_add_u32 s77, s77, s62\n"
    "s_add_u32 s6, s56, 6\n"
    "s_and_b32 s6, s6, 7\n"
    "s_lshl_b32 s70, s6, 7\n"
    "s_add_u32 s70, s70, s61\n"
    "s_mul_i32 s78, s6, 229376\n"
    "s_add_u32 s78, s78, s62\n"
    "buffer_load_dwordx4 v195, s[20:23], s64 offen offset:3072 lds\n"
    "s_add_u32 s6, s56, 7\n"
    "s_and_b32 s6, s6, 7\n"
    "s_lshl_b32 s71, s6, 7\n"
    "s_add_u32 s71, s71, s61\n"
    "s_mul_i32 s79, s6, 229376\n"
    "s_add_u32 s79, s79, s62\n"
    "s_lshl2_add_u32 s54, s49, s59\n"
    "s_xor_b32 s55, s60, 3\n"
    "s_mov_b32 s87, 0\n"
    "s_mov_b32 s36, s18\n"
    "s_and_b32 s37, s19, 0xffff\n"
    "s_mov_b32 s40, s16\n"
    "s_add_u32 m0, s63, 16384\n"
    "s_nop 0\n"
    "buffer_load_dwordx4 v196, s[24:27], s72 offen offset:0 lds\n"
    "s_and_b32 s41, s17, 0xffff\n"
    "s_mov_b32 s34, 1835008\n"
    "s_mov_b32 s35, 0x20000\n"
    "s_mov_b32 s38, 33554432\n"
    "s_mov_b32 s39, 0x20000\n"
    "s_mov_b32 s42, 2097152\n"
    "s_mov_b32 s43, 0x20000\n"
    "v_lshlrev_b32 v7, 1, v6\n"
    "v_xor_b32 v7, v5, v7\n"
    "s_and_b32 s92, s51, 1\n"
    "s_lshr_b32 s93, s51, 1\n"
    "s_lshl_b32 s61, s93, 17\n"
    "buffer_load_dwordx4 v197, s[24:27], s72 offen offset:1024 lds\n"
    "s_lshl_b32 s62, s92, 15\n"
    "s_add_u32 s61, s61, s62\n"
    "v_lshl_add_u32 v8, v4, 10, s61\n"
    "v_lshl_add_u32 v198, v7, 4, v8\n"
    "v_add_u32 v199, 7168, v198\n"
    "v_add_u32 v200, 14336, v198\n"
    "v_add_u32 v201, 21504, v198\n"
    "s_lshl3_add_u32 s61, s54, s55\n"
    "s_lshl4_add_u32 s61, s61, s92\n"
    "s_lshl_b32 s94, s61, 7\n"
    "s_add_u32 s94, s94, 8388608\n"
    "s_add_u32 s96, s18, s94\n"
    "s_addc_u32 s97, s19, 0\n"
    "buffer_load_dwordx4 v196, s[28:31], s72 offen offset:2048 lds\n"
    "s_mul_i32 s61, s93, 524288\n"
    "s_load_dword s95, s[96:97], s61 offset:0x0 glc\n"
    "s_load_dword s95, s[96:97], s61 offset:0x100 glc\n"
    "s_load_dword s95, s[96:97], s61 offset:0x200 glc\n"
    "s_load_dword s95, s[96:97], s61 offset:0x300 glc\n"
    "s_load_dword s95, s[96:97], s61 offset:0x400 glc\n"
    "s_load_dword s95, s[96:97], s61 offset:0x500 glc\n"
    "s_load_dword s95, s[96:97], s61 offset:0x600 glc\n"
    "s_lshl1_add_u32 s61, s54, s93\n"
    "s_mul_i32 s61, s61, 229376\n"
    "s_lshl_b32 s62, s92, 14\n"
    "s_add_u32 s61, s61, s62\n"
    "buffer_load_dwordx4 v197, s[28:31], s72 offen offset:3072 lds\n"
    "s_add_u32 s61, s61, 16777216\n"
    "s_lshl_b32 s62, s55, 7\n"
    "s_add_u32 s80, s62, 0\n"
    "s_add_u32 s8, s61, 0\n"
    "s_add_u32 s81, s62, 262144\n"
    "s_add_u32 s9, s61, 32768\n"
    "s_add_u32 s82, s62, 524288\n"
    "s_add_u32 s10, s61, 65536\n"
    "s_add_u32 s83, s62, 786432\n"
    "s_add_u32 s11, s61, 98304\n"
    "s_add_u32 s84, s62, 1048576\n"
    "s_add_u32 s12, s61, 131072\n"
    "s_add_u32 s98, s72, 128\n"
    "s_add_u32 m0, s63, 32768\n"
    "s_nop 0\n"
    "buffer_load_dwordx4 v196, s[24:27], s98 offen offset:0 lds\n"
    "s_add_u32 s85, s62, 1310720\n"
    "s_add_u32 s13, s61, 163840\n"
    "s_add_u32 s86, s62, 1572864\n"
    "s_add_u32 s14, s61, 196608\n"
    "s_lshl1_add_u32 s61, s52, s93\n"
    "s_mul_i32 s90, s61, 458752\n"
    "s_lshl2_add_u32 s61, s53, s92\n"
    "s_lshl_b32 s61, s61, 12\n"
    "s_add_u32 s90, s90, s61\n"
    "s_add_u32 s90, s90, 16777216\n"
    "s_add_u32 s91, s90, 229376\n"
    "s_lshl_b32 s89, s52, 8\n"
    "buffer_load_dwordx4 v197, s[24:27], s98 offen offset:1024 lds\n"
    "s_add_u32 s89, s89, s53\n"
    "s_lshl_b32 s89, s89, 7\n"
    "s_add_u32 s89, s89, 8388608\n"
    "s_cmp_lt_u32 s50, 4\n"
    "s_cselect_b32 s88, 1, s51\n"
    "v_bfe_u32 v9, v232, 2, 2\n"
    "v_and_b32 v10, 3, v232\n"
    "v_bfe_u32 v11, v232, 3, 2\n"
    "v_lshlrev_b32 v12, 5, v11\n"
    "v_lshl_add_u32 v12, v10, 3, v12\n"
    "v_lshl_add_u32 v12, v9, 7, v12\n"
    "s_lshl_b32 s61, s92, 6\n"
    "buffer_load_dwordx4 v196, s[28:31], s98 offen offset:2048 lds\n"
    "v_xor_b32 v12, s61, v12\n"
    "v_and_b32 v13, 1, v6\n"
    "v_lshl_add_u32 v13, v13, 10, v12\n"
    "v_lshrrev_b32 v14, 1, v6\n"
    "v_lshl_add_u32 v202, v14, 12, v13\n"
    "v_xor_b32 v203, 32, v202\n"
    "v_add_u32 v204, 65536, v202\n"
    "v_add_u32 v205, 65536, v203\n"
    "v_add_u32 v206, 131072, v202\n"
    "v_add_u32 v207, 131072, v203\n"
    "s_lshl_b32 s61, s92, 13\n"
    "v_lshl_add_u32 v13, v6, 9, v12\n"
    "buffer_load_dwordx4 v197, s[28:31], s98 offen offset:3072 lds\n"
    "v_add_u32 v214, s61, v13\n"
    "v_xor_b32 v215, 32, v214\n"
    "v_xor_b32 v216, 64, v214\n"
    "v_xor_b32 v217, 96, v214\n"
    "v_add_u32 v218, 65536, v214\n"
    "v_add_u32 v219, 65536, v215\n"
    "v_add_u32 v220, 65536, v216\n"
    "v_add_u32 v221, 65536, v217\n"
    "v_add_u32 v222, 131072, v214\n"
    "v_add_u32 v223, 131072, v215\n"
    "v_add_u32 v224, 131072, v216\n"
    "v_add_u32 v225, 131072, v217\n"
    "s_add_u32 m0, s63, 49152\n"
    "s_nop 0\n"
    "buffer_load_dwordx4 v192, s[20:23], s65 offen offset:0 lds\n"
    "v_and_b32 v9, 15, v232\n"
    "v_lshrrev_b32 v10, 1, v9\n"
    "v_xor_b32 v10, v10, v6\n"
    "s_lshl_b32 s61, s93, 13\n"
    "v_lshl_add_u32 v9, v9, 7, s61\n"
    "v_lshl_add_u32 v208, v10, 4, v9\n"
    "v_xor_b32 v209, 64, v208\n"
    "v_add_u32 v210, 65536, v208\n"
    "v_add_u32 v211, 65536, v209\n"
    "v_add_u32 v212, 131072, v208\n"
    "v_add_u32 v213, 131072, v209\n"
    "v_lshlrev_b32 v226, 4, v232\n"
    "buffer_load_dwordx4 v193, s[20:23], s65 offen offset:1024 lds\n"
    "v_lshrrev_b32 v9, 2, v232\n"
    "v_and_b32 v10, 48, v226\n"
    "v_lshl_add_u32 v227, v9, 11, v10\n"
    "v_lshlrev_b32 v9, 4, v5\n"
    "v_lshl_add_u32 v246, v4, 8, v9\n"
    "v_add_u32 v247, 4096, v246\n"
    "buffer_load_dwordx4 v194, s[20:23], s65 offen offset:2048 lds\n"
    "buffer_load_dwordx4 v195, s[20:23], s65 offen offset:3072 lds\n"
    "s_waitcnt lgkmcnt(0)\n"
    "s_waitcnt vmcnt(8)\n"
    "s_barrier\n"
    "ds_read_b64_tr_b16 v[128:129], v202 offset:16384\n"
    "ds_read_b64_tr_b16 v[130:131], v202 offset:16896\n"
    "ds_read_b128 a[0:3], v208 offset:0\n"
    "ds_read_b64_tr_b16 v[132:133], v202 offset:24576\n"
    "ds_read_b64_tr_b16 v[134:135], v202 offset:25088\n"
    "ds_read_b128 a[16:19], v209 offset:0\n"
    "ds_read_b128 a[4:7], v208 offset:2048\n"
    "ds_read_b128 a[20:23], v209 offset:2048\n"
    "ds_read_b128 a[8:11], v208 offset:4096\n"
    "ds_read_b128 a[24:27], v209 offset:4096\n"
    "s_waitcnt lgkmcnt(0)\n"
    "v_mfma_f32_16x16x32_bf16 v[0:3], v[128:131], a[0:3], 0\n"
    "ds_read_b128 a[12:15], v208 offset:6144\n"
    "s_add_u32 m0, s63, 65536\n"
    "s_mul_i32 s44, s46, 0x4c957f2d\n"
    "v_mfma_f32_16x16x32_bf16 v[0:3], v[132:135], a[16:19], v[0:3]\n"
    "ds_read_b128 a[28:31], v209 offset:6144\n"
    "s_mul_hi_u32 s45, s46, 0x4c957f2d\n"
    "v_mfma_f32_16x16x32_bf16 v[4:7], v[128:131], a[4:7], 0\n"
    "ds_read_b64_tr_b16 v[136:137], v203 offset:16384\n"
    "s_mul_i32 s57, s46, 0x5851f42d\n"
    "v_mfma_f32_16x16x32_bf16 v[4:7], v[132:135], a[20:23], v[4:7]\n"
    "ds_read_b64_tr_b16 v[138:139], v203 offset:16896\n"
    "s_add_u32 s45, s45, s57\n"
    "v_mfma_f32_16x16x32_bf16 v[8:11], v[128:131], a[8:11], 0\n"
    "ds_read_b64_tr_b16 v[140:141], v203 offset:24576\n"
    "ds_read_b64_tr_b16 v[142:143], v203 offset:25088\n"
    "buffer_load_dwordx4 v196, s[24:27], s73 offen offset:0 lds\n"
    "v_mfma_f32_16x16x32_bf16 v[8:11], v[132:135], a[24:27], v[8:11]\n"
    "ds_read_b64_tr_b16 v[144:145], v202 offset:18432\n"
    "s_mul_i32 s57, s47, 0x4c957f2d\n"
    "s_waitcnt lgkmcnt(5)\n"
    "v_mfma_f32_16x16x32_bf16 v[12:15], v[128:131], a[12:15], 0\n"
    "buffer_load_dwordx4 v197, s[24:27], s73 offen offset:1024 lds\n"
    "s_add_u32 s45, s45, s57\n"
    "v_mfma_f32_16x16x32_bf16 v[12:15], v[132:135], a[28:31], v[12:15]\n"
    "ds_read_b64_tr_b16 v[146:147], v202 offset:18944\n"
    "s_add_u32 s44, s44, 0xf767814f\ns_addc_u32 s45, s45, 0x14057b7e\n"
    "s_waitcnt lgkmcnt(4)\n"
    "v_mfma_f32_16x16x32_bf16 v[16:19], v[136:139], a[0:3], 0\n"
    "buffer_load_dwordx4 v196, s[28:31], s73 offen offset:2048 lds\n"
    "v_mov_b32 v228, s44\n"
    "s_waitcnt lgkmcnt(2)\n"
    "v_mfma_f32_16x16x32_bf16 v[16:19], v[140:143], a[16:19], v[16:19]\n"
    "ds_read_b64_tr_b16 v[148:149], v202 offset:26624\n"
    "v_mov_b32 v229, s45\n"
    "v_mfma_f32_16x16x32_bf16 v[20:23], v[136:139], a[4:7], 0\n"
    "ds_read_b64_tr_b16 v[150:151], v202 offset:27136\n"
    "v_mov_b32 v230, s44\n"
    "v_mfma_f32_16x16x32_bf16 v[20:23], v[140:143], a[20:23], v[20:23]\n"
    "buffer_load_dwordx4 v197, s[28:31], s73 offen offset:3072 lds\n"
    "v_mov_b32 v231, s45\n"
    "v_mfma_f32_16x16x32_bf16 v[24:27], v[136:139], a[8:11], 0\n"
    "ds_read_b64_tr_b16 v[152:153], v203 offset:18432\n"
    "s_add_u32 s99, s73, 128\n"
    "v_mfma_f32_16x16x32_bf16 v[24:27], v[140:143], a[24:27], v[24:27]\n"
    "ds_read_b64_tr_b16 v[154:155], v203 offset:18944\n"
    "s_add_u32 m0, s63, 81920\n"
    "v_mfma_f32_16x16x32_bf16 v[28:31], v[136:139], a[12:15], 0\n"
    "buffer_load_dwordx4 v196, s[24:27], s99 offen offset:0 lds\n"
    "v_mfma_f32_16x16x32_bf16 v[28:31], v[140:143], a[28:31], v[28:31]\n"
    "ds_read_b64_tr_b16 v[156:157], v203 offset:26624\n"
    "s_waitcnt lgkmcnt(3)\n"
    "v_mfma_f32_16x16x32_bf16 v[32:35], v[144:147], a[0:3], 0\n"
    "ds_read_b64_tr_b16 v[158:159], v203 offset:27136\n"
    "v_mfma_f32_16x16x32_bf16 v[32:35], v[148:151], a[16:19], v[32:35]\n"
    "buffer_load_dwordx4 v197, s[24:27], s99 offen offset:1024 lds\n"
    "v_mfma_f32_16x16x32_bf16 v[36:39], v[144:147], a[4:7], 0\n"
    "v_mfma_f32_16x16x32_bf16 v[36:39], v[148:151], a[20:23], v[36:39]\n"
    "v_mfma_f32_16x16x32_bf16 v[40:43], v[144:147], a[8:11], 0\n"
    "s_waitcnt vmcnt(10)\n"
    "s_barrier\n"
    "v_mfma_f32_16x16x32_bf16 v[40:43], v[148:151], a[24:27], v[40:43]\n"
    "buffer_load_dwordx4 v196, s[28:31], s99 offen offset:2048 lds\n"
    "v_mfma_f32_16x16x32_bf16 v[44:47], v[144:147], a[12:15], 0\n"
    "ds_read_b64_tr_b16 v[160:161], v202 offset:32768\n"
    "v_mfma_f32_16x16x32_bf16 v[44:47], v[148:151], a[28:31], v[44:47]\n"
    "buffer_load_dwordx4 v197, s[28:31], s99 offen offset:3072 lds\n"
    "s_waitcnt lgkmcnt(1)\n"
    "v_mfma_f32_16x16x32_bf16 v[48:51], v[152:155], a[0:3], 0\n"
    "ds_read_b64_tr_b16 v[162:163], v202 offset:33280\n"
    "v_mfma_f32_16x16x32_bf16 v[48:51], v[156:159], a[16:19], v[48:51]\n"
    "ds_read_b64_tr_b16 v[164:165], v202 offset:40960\n"
    "s_add_u32 m0, s63, 0\n"
    "v_mfma_f32_16x16x32_bf16 v[52:55], v[152:155], a[4:7], 0\n"
    "ds_read_b64_tr_b16 v[166:167], v202 offset:41472\n"
    "v_mfma_f32_16x16x32_bf16 v[52:55], v[156:159], a[20:23], v[52:55]\n"
    "buffer_load_dwordx4 v198, s[32:35], s80 offen offset:0 lds\n"
    "v_mfma_f32_16x16x32_bf16 v[56:59], v[152:155], a[8:11], 0\n"
    "ds_read_b64_tr_b16 v[168:169], v203 offset:32768\n"
    "v_mfma_f32_16x16x32_bf16 v[56:59], v[156:159], a[24:27], v[56:59]\n"
    "ds_read_b64_tr_b16 v[170:171], v203 offset:33280\n"
    "v_mfma_f32_16x16x32_bf16 v[60:63], v[152:155], a[12:15], 0\n"
    "buffer_load_dwordx4 v199, s[32:35], s80 offen offset:1024 lds\n"
    "v_mfma_f32_16x16x32_bf16 v[60:63], v[156:159], a[28:31], v[60:63]\n"
    "ds_read_b64_tr_b16 v[172:173], v203 offset:40960\n"
    "s_waitcnt lgkmcnt(3)\n"
    "v_mfma_f32_16x16x32_bf16 v[64:67], v[160:163], a[0:3], 0\n"
    "ds_read_b64_tr_b16 v[174:175], v203 offset:41472\n"
    "v_mfma_f32_16x16x32_bf16 v[64:67], v[164:167], a[16:19], v[64:67]\n"
    "buffer_load_dwordx4 v200, s[32:35], s80 offen offset:2048 lds\n"
    "v_mfma_f32_16x16x32_bf16 v[68:71], v[160:163], a[4:7], 0\n"
    "ds_read_b64_tr_b16 v[176:177], v202 offset:34816\n"
    "v_mfma_f32_16x16x32_bf16 v[68:71], v[164:167], a[20:23], v[68:71]\n"
    "ds_read_b64_tr_b16 v[178:179], v202 offset:35328\n"
    "v_mfma_f32_16x16x32_bf16 v[72:75], v[160:163], a[8:11], 0\n"
    "buffer_load_dwordx4 v201, s[32:35], s80 offen offset:3072 lds\n"
    "v_mfma_f32_16x16x32_bf16 v[72:75], v[164:167], a[24:27], v[72:75]\n"
    "ds_read_b64_tr_b16 v[180:181], v202 offset:43008\n"
    "s_add_u32 m0, s63, 98304\n"
    "v_mfma_f32_16x16x32_bf16 v[76:79], v[160:163], a[12:15], 0\n"
    "ds_read_b64_tr_b16 v[182:183], v202 offset:43520\n"
    "v_mfma_f32_16x16x32_bf16 v[76:79], v[164:167], a[28:31], v[76:79]\n"
    "buffer_load_dwordx4 v192, s[20:23], s66 offen offset:0 lds\n"
    "s_waitcnt lgkmcnt(2)\n"
    "v_mfma_f32_16x16x32_bf16 v[80:83], v[168:171], a[0:3], 0\n"
    "s_cmp_lg_u32 s51, 0\n"
    "s_cbranch_scc1 .Lg_nohello_1\n"
    "s_lshl_b32 s57, s48, 7\n"
    "s_add_u32 s57, s57, 10485760\n"
    "s_mov_b64 exec, 15\n"
    "buffer_store_dwordx4 v[228:231], v226, s[36:39], s57 offen sc1\n"
    "s_mov_b64 exec, -1\n"
    ".Lg_nohello_1:\n"
    "v_mfma_f32_16x16x32_bf16 v[80:83], v[172:175], a[16:19], v[80:83]\n"
    "ds_read_b64_tr_b16 v[184:185], v203 offset:34816\n"
    "v_mfma_f32_16x16x32_bf16 v[84:87], v[168:171], a[4:7], 0\n"
    "ds_read_b64_tr_b16 v[186:187], v203 offset:35328\n"
    "v_mfma_f32_16x16x32_bf16 v[84:87], v[172:175], a[20:23], v[84:87]\n"
    "buffer_load_dwordx4 v193, s[20:23], s66 offen offset:1024 lds\n"
    "v_mfma_f32_16x16x32_bf16 v[88:91], v[168:171], a[8:11], 0\n"
    "ds_read_b64_tr_b16 v[188:189], v203 offset:43008\n"
    "v_mfma_f32_16x16x32_bf16 v[88:91], v[172:175], a[24:27], v[88:91]\n"
    "ds_read_b64_tr_b16 v[190:191], v203 offset:43520\n"
    "v_mfma_f32_16x16x32_bf16 v[92:95], v[168:171], a[12:15], 0\n"
    "buffer_load_dwordx4 v194, s[20:23], s66 offen offset:2048 lds\n"
    "v_mfma_f32_16x16x32_bf16 v[92:95], v[172:175], a[28:31], v[92:95]\n"
    "v_mfma_f32_16x16x32_bf16 v[96:99], v[176:179], a[0:3], 0\n"
    "s_waitcnt lgkmcnt(1)\n"
    "v_mfma_f32_16x16x32_bf16 v[96:99], v[180:183], a[16:19], v[96:99]\n"
    "buffer_load_dwordx4 v195, s[20:23], s66 offen offset:3072 lds\n"
    "v_mfma_f32_16x16x32_bf16 v[100:103], v[176:179], a[4:7], 0\n"
    "s_add_u32 m0, s63, 114688\n"
    "v_mfma_f32_16x16x32_bf16 v[100:103], v[180:183], a[20:23], v[100:103]\n"
    "v_mfma_f32_16x16x32_bf16 v[104:107], v[176:179], a[8:11], 0\n"
    "s_waitcnt vmcnt(12)\n"
    "s_barrier\n"
    "v_mfma_f32_16x16x32_bf16 v[104:107], v[180:183], a[24:27], v[104:107]\n"
    "buffer_load_dwordx4 v196, s[24:27], s74 offen offset:0 lds\n"
    "v_mfma_f32_16x16x32_bf16 v[108:111], v[176:179], a[12:15], 0\n"
    "ds_read_b64_tr_b16 v[128:129], v204 offset:0\n"
    "v_mfma_f32_16x16x32_bf16 v[108:111], v[180:183], a[28:31], v[108:111]\n"
    "buffer_load_dwordx4 v197, s[24:27], s74 offen offset:1024 lds\n"
    "v_mfma_f32_16x16x32_bf16 v[112:115], v[184:187], a[0:3], 0\n"
    "ds_read_b64_tr_b16 v[130:131], v204 offset:512\n"
    "s_waitcnt lgkmcnt(2)\n"
    "v_mfma_f32_16x16x32_bf16 v[112:115], v[188:191], a[16:19], v[112:115]\n"
    "ds_read_b128 a[32:35], v208 offset:49152\n"
    "v_mfma_f32_16x16x32_bf16 v[116:119], v[184:187], a[4:7], 0\n"
    "ds_read_b64_tr_b16 v[132:133], v204 offset:8192\n"
    "v_mfma_f32_16x16x32_bf16 v[116:119], v[188:191], a[20:23], v[116:119]\n"
    "ds_read_b64_tr_b16 v[134:135], v204 offset:8704\n"
    "v_mfma_f32_16x16x32_bf16 v[120:123], v[184:187], a[8:11], 0\n"
    "ds_read_b128 a[48:51], v209 offset:49152\n"
    "v_mfma_f32_16x16x32_bf16 v[120:123], v[188:191], a[24:27], v[120:123]\n"
    "ds_read_b128 a[36:39], v208 offset:51200\n"
    "v_mfma_f32_16x16x32_bf16 v[124:127], v[184:187], a[12:15], 0\n"
    "ds_read_b128 a[52:55], v209 offset:51200\n"
    "buffer_load_dwordx4 v196, s[28:31], s74 offen offset:2048 lds\n"
    "v_mfma_f32_16x16x32_bf16 v[124:127], v[188:191], a[28:31], v[124:127]\n"
    "ds_read_b128 a[40:43], v208 offset:53248\n"
    "s_waitcnt lgkmcnt(4)\n"
    "v_mfma_f32_16x16x32_bf16 v[0:3], v[128:131], a[32:35], v[0:3]\n"
    "ds_read_b128 a[56:59], v209 offset:53248\n"
    "s_waitcnt lgkmcnt(4)\n"
    "v_mfma_f32_16x16x32_bf16 v[0:3], v[132:135], a[48:51], v[0:3]\n"
    "ds_read_b128 a[44:47], v208 offset:55296\n"
    "buffer_load_dwordx4 v197, s[28:31], s74 offen offset:3072 lds\n"
    "s_waitcnt lgkmcnt(4)\n"
    "v_mfma_f32_16x16x32_bf16 v[4:7], v[128:131], a[36:39], v[4:7]\n"
    "ds_read_b128 a[60:63], v209 offset:55296\n"
    "s_add_u32 m0, s63, 16384\n"
    "s_waitcnt lgkmcnt(4)\n"
    "v_mfma_f32_16x16x32_bf16 v[4:7], v[132:135], a[52:55], v[4:7]\n"
    "ds_read_b64_tr_b16 v[136:137], v205 offset:0\n"
    "ds_read_b64_tr_b16 v[138:139], v205 offset:512\n"
    "s_waitcnt lgkmcnt(5)\n"
    "v_mfma_f32_16x16x32_bf16 v[8:11], v[128:131], a[40:43], v[8:11]\n"
    "ds_read_b64_tr_b16 v[140:141], v205 offset:8192\n"
    "ds_read_b64_tr_b16 v[142:143], v205 offset:8704\n"
    "buffer_load_dwordx4 v198, s[32:35], s81 offen offset:0 lds\n"
    "s_waitcnt lgkmcnt(6)\n"
    "v_mfma_f32_16x16x32_bf16 v[8:11], v[132:135], a[56:59], v[8:11]\n"
    "ds_read_b64_tr_b16 v[144:145], v204 offset:2048\n"
    "s_waitcnt lgkmcnt(6)\n"
    "v_mfma_f32_16x16x32_bf16 v[12:15], v[128:131], a[44:47], v[12:15]\n"
    "buffer_load_dwordx4 v199, s[32:35], s81 offen offset:1024 lds\n"
    "s_waitcnt lgkmcnt(5)\n"
    "v_mfma_f32_16x16x32_bf16 v[12:15], v[132:135], a[60:63], v[12:15]\n"
    "ds_read_b64_tr_b16 v[146:147], v204 offset:2560\n"
    "s_waitcnt lgkmcnt(4)\n"
    "v_mfma_f32_16x16x32_bf16 v[16:19], v[136:139], a[32:35], v[16:19]\n"
    "buffer_load_dwordx4 v200, s[32:35], s81 offen offset:2048 lds\n"
    "s_waitcnt lgkmcnt(2)\n"
    "v_mfma_f32_16x16x32_bf16 v[16:19], v[140:143], a[48:51], v[16:19]\n"
    "ds_read_b64_tr_b16 v[148:149], v204 offset:10240\n"
    "v_mfma_f32_16x16x32_bf16 v[20:23], v[136:139], a[36:39], v[20:23]\n"
    "ds_read_b64_tr_b16 v[150:151], v204 offset:10752\n"
    "v_mfma_f32_16x16x32_bf16 v[20:23], v[140:143], a[52:55], v[20:23]\n"
    "buffer_load_dwordx4 v201, s[32:35], s81 offen offset:3072 lds\n"
    "v_mfma_f32_16x16x32_bf16 v[24:27], v[136:139], a[40:43], v[24:27]\n"
    "ds_read_b64_tr_b16 v[152:153], v205 offset:2048\n"
    "s_add_u32 m0, s63, 131072\n"
    "v_mfma_f32_16x16x32_bf16 v[24:27], v[140:143], a[56:59], v[24:27]\n"
    "ds_read_b64_tr_b16 v[154:155], v205 offset:2560\n"
    "v_mfma_f32_16x16x32_bf16 v[28:31], v[136:139], a[44:47], v[28:31]\n"
    "buffer_load_dwordx4 v192, s[20:23], s67 offen offset:0 lds\n"
    "v_mfma_f32_16x16x32_bf16 v[28:31], v[140:143], a[60:63], v[28:31]\n"
    "ds_read_b64_tr_b16 v[156:157], v205 offset:10240\n"
    "s_waitcnt lgkmcnt(3)\n"
    "v_mfma_f32_16x16x32_bf16 v[32:35], v[144:147], a[32:35], v[32:35]\n"
    "ds_read_b64_tr_b16 v[158:159], v205 offset:10752\n"
    "v_mfma_f32_16x16x32_bf16 v[32:35], v[148:151], a[48:51], v[32:35]\n"
    "buffer_load_dwordx4 v193, s[20:23], s67 offen offset:1024 lds\n"
    "v_mfma_f32_16x16x32_bf16 v[36:39], v[144:147], a[36:39], v[36:39]\n"
    "v_mfma_f32_16x16x32_bf16 v[36:39], v[148:151], a[52:55], v[36:39]\n"
    "v_mfma_f32_16x16x32_bf16 v[40:43], v[144:147], a[40:43], v[40:43]\n"
    "s_waitcnt vmcnt(12)\n"
    "s_barrier\n"
    "ds_read_b64_tr_b16 v[160:161], v204 offset:16384\n"
    "v_mfma_f32_16x16x32_bf16 v[40:43], v[148:151], a[56:59], v[40:43]\n"
    "buffer_load_dwordx4 v194, s[20:23], s67 offen offset:2048 lds\n"
    "ds_read_b64_tr_b16 v[162:163], v204 offset:16896\n"
    "v_mfma_f32_16x16x32_bf16 v[44:47], v[144:147], a[44:47], v[44:47]\n"
    "ds_read_b64_tr_b16 v[164:165], v204 offset:24576\n"
    "v_mfma_f32_16x16x32_bf16 v[44:47], v[148:151], a[60:63], v[44:47]\n"
    "buffer_load_dwordx4 v195, s[20:23], s67 offen offset:3072 lds\n"
    "ds_read_b64_tr_b16 v[166:167], v204 offset:25088\n"
    "s_waitcnt lgkmcnt(4)\n"
    "v_mfma_f32_16x16x32_bf16 v[48:51], v[152:155], a[32:35], v[48:51]\n"
    "ds_read_b64_tr_b16 v[168:169], v205 offset:16384\n"
    "v_mfma_f32_16x16x32_bf16 v[48:51], v[156:159], a[48:51], v[48:51]\n"
    "ds_read_b64_tr_b16 v[170:171], v205 offset:16896\n"
    "s_add_u32 m0, s63, 32768\n"
    "v_mfma_f32_16x16x32_bf16 v[52:55], v[152:155], a[36:39], v[52:55]\n"
    "ds_read_b64_tr_b16 v[172:173], v205 offset:24576\n"
    "v_mfma_f32_16x16x32_bf16 v[52:55], v[156:159], a[52:55], v[52:55]\n"
    "buffer_load_dwordx4 v196, s[24:27], s75 offen offset:0 lds\n"
    "ds_read_b64_tr_b16 v[174:175], v205 offset:25088\n"
    "v_mfma_f32_16x16x32_bf16 v[56:59], v[152:155], a[40:43], v[56:59]\n"
    "ds_read_b64_tr_b16 a[192:193], v214 offset:0\n"
    "v_mfma_f32_16x16x32_bf16 v[56:59], v[156:159], a[56:59], v[56:59]\n"
    "ds_read_b64_tr_b16 a[194:195], v214 offset:2048\n"
    "v_mfma_f32_16x16x32_bf16 v[60:63], v[152:155], a[44:47], v[60:63]\n"
    "buffer_load_dwordx4 v197, s[24:27], s75 offen offset:1024 lds\n"
    "ds_read_b64_tr_b16 a[196:197], v215 offset:0\n"
    "v_mfma_f32_16x16x32_bf16 v[60:63], v[156:159], a[60:63], v[60:63]\n"
    "ds_read_b64_tr_b16 a[198:199], v215 offset:2048\n"
    "s_waitcnt lgkmcnt(4)\n"
    "v_mfma_f32_16x16x32_bf16 v[64:67], v[160:163], a[32:35], v[64:67]\n"
    "ds_read_b64_tr_b16 v[176:177], v204 offset:18432\n"
    "v_mfma_f32_16x16x32_bf16 v[64:67], v[164:167], a[48:51], v[64:67]\n"
    "buffer_load_dwordx4 v196, s[28:31], s75 offen offset:2048 lds\n"
    "ds_read_b64_tr_b16 v[178:179], v204 offset:18944\n"
    "v_mfma_f32_16x16x32_bf16 v[68:71], v[160:163], a[36:39], v[68:71]\n"
    "ds_read_b64_tr_b16 v[180:181], v204 offset:26624\n"
    "v_mfma_f32_16x16x32_bf16 v[68:71], v[164:167], a[52:55], v[68:71]\n"
    "ds_read_b64_tr_b16 v[182:183], v204 offset:27136\n"
    "v_mfma_f32_16x16x32_bf16 v[72:75], v[160:163], a[40:43], v[72:75]\n"
    "buffer_load_dwordx4 v197, s[28:31], s75 offen offset:3072 lds\n"
    "ds_read_b64_tr_b16 a[200:201], v216 offset:0\n"
    "v_mfma_f32_16x16x32_bf16 v[72:75], v[164:167], a[56:59], v[72:75]\n"
    "ds_read_b64_tr_b16 a[202:203], v216 offset:2048\n"
    "s_add_u32 m0, s63, 49152\n"
    "v_mfma_f32_16x16x32_bf16 v[76:79], v[160:163], a[44:47], v[76:79]\n"
    "ds_read_b64_tr_b16 a[204:205], v217 offset:0\n"
    "v_mfma_f32_16x16x32_bf16 v[76:79], v[164:167], a[60:63], v[76:79]\n"
    "buffer_load_dwordx4 v192, s[20:23], s68 offen offset:0 lds\n"
    "ds_read_b64_tr_b16 a[206:207], v217 offset:2048\n"
    "v_mfma_f32_16x16x32_bf16 v[80:83], v[168:171], a[32:35], v[80:83]\n"
    "ds_read_b64_tr_b16 v[184:185], v205 offset:18432\n"
    "v_mfma_f32_16x16x32_bf16 v[80:83], v[172:175], a[48:51], v[80:83]\n"
    "ds_read_b64_tr_b16 v[186:187], v205 offset:18944\n"
    "v_mfma_f32_16x16x32_bf16 v[84:87], v[168:171], a[36:39], v[84:87]\n"
    "ds_read_b64_tr_b16 v[188:189], v205 offset:26624\n"
    "v_mfma_f32_16x16x32_bf16 v[84:87], v[172:175], a[52:55], v[84:87]\n"
    "buffer_load_dwordx4 v193, s[20:23], s68 offen offset:1024 lds\n"
    "ds_read_b64_tr_b16 v[190:191], v205 offset:27136\n"
    "v_mfma_f32_16x16x32_bf16 v[88:91], v[168:171], a[40:43], v[88:91]\n"
    "ds_read_b64_tr_b16 a[208:209], v214 offset:4096\n"
    "v_mfma_f32_16x16x32_bf16 v[88:91], v[172:175], a[56:59], v[88:91]\n"
    "ds_read_b64_tr_b16 a[210:211], v214 offset:6144\n"
    "v_mfma_f32_16x16x32_bf16 v[92:95], v[168:171], a[44:47], v[92:95]\n"
    "buffer_load_dwordx4 v194, s[20:23], s68 offen offset:2048 lds\n"
    "ds_read_b64_tr_b16 a[212:213], v215 offset:4096\n"
    "v_mfma_f32_16x16x32_bf16 v[92:95], v[172:175], a[60:63], v[92:95]\n"
    "ds_read_b64_tr_b16 a[214:215], v215 offset:6144\n"
    "s_waitcnt lgkmcnt(4)\n"
    "v_mfma_f32_16x16x32_bf16 v[96:99], v[176:179], a[32:35], v[96:99]\n"
    "ds_read_b64_tr_b16 a[216:217], v216 offset:4096\n"
    "v_mfma_f32_16x16x32_bf16 v[96:99], v[180:183], a[48:51], v[96:99]\n"
    "buffer_load_dwordx4 v195, s[20:23], s68 offen offset:3072 lds\n"
    "ds_read_b64_tr_b16 a[218:219], v216 offset:6144\n"
    "v_mfma_f32_16x16x32_bf16 v[100:103], v[176:179], a[36:39], v[100:103]\n"
    "ds_read_b64_tr_b16 a[220:221], v217 offset:4096\n"
    "s_add_u32 m0, s63, 147456\n"
    "v_mfma_f32_16x16x32_bf16 v[100:103], v[180:183], a[52:55], v[100:103]\n"
    "ds_read_b64_tr_b16 a[222:223], v217 offset:6144\n"
    "v_mfma_f32_16x16x32_bf16 v[104:107], v[176:179], a[40:43], v[104:107]\n"
    "s_waitcnt vmcnt(12)\n"
    "s_barrier\n"
    "ds_read_b64_tr_b16 v[128:129], v204 offset:49152\n"
    "v_mfma_f32_16x16x32_bf16 v[104:107], v[180:183], a[56:59], v[104:107]\n"
    "buffer_load_dwordx4 v196, s[24:27], s76 offen offset:0 lds\n"
    "ds_read_b64_tr_b16 v[130:131], v204 offset:49664\n"
    "v_mfma_f32_16x16x32_bf16 v[108:111], v[176:179], a[44:47], v[108:111]\n"
    "ds_read_b128 a[64:67], v210 offset:32768\n"
    "v_mfma_f32_16x16x32_bf16 v[108:111], v[180:183], a[60:63], v[108:111]\n"
    "buffer_load_dwordx4 v197, s[24:27], s76 offen offset:1024 lds\n"
    "ds_read_b64_tr_b16 v[132:133], v204 offset:57344\n"
    "v_mfma_f32_16x16x32_bf16 v[112:115], v[184:187], a[32:35], v[112:115]\n"
    "ds_read_b64_tr_b16 v[134:135], v204 offset:57856\n"
    "v_mfma_f32_16x16x32_bf16 v[112:115], v[188:191], a[48:51], v[112:115]\n"
    "ds_read_b128 a[80:83], v211 offset:32768\n"
    "v_mfma_f32_16x16x32_bf16 v[116:119], v[184:187], a[36:39], v[116:119]\n"
    "ds_read_b128 a[68:71], v210 offset:34816\n"
    "v_mfma_f32_16x16x32_bf16 v[116:119], v[188:191], a[52:55], v[116:119]\n"
    "ds_read_b128 a[84:87], v211 offset:34816\n"
    "v_mfma_f32_16x16x32_bf16 v[120:123], v[184:187], a[40:43], v[120:123]\n"
    "ds_read_b128 a[72:75], v210 offset:36864\n"
    "v_mfma_f32_16x16x32_bf16 v[120:123], v[188:191], a[56:59], v[120:123]\n"
    "ds_read_b128 a[88:91], v211 offset:36864\n"
    "v_mfma_f32_16x16x32_bf16 v[124:127], v[184:187], a[44:47], v[124:127]\n"
    "ds_read_b128 a[76:79], v210 offset:38912\n"
    "buffer_load_dwordx4 v196, s[28:31], s76 offen offset:2048 lds\n"
    "v_mfma_f32_16x16x32_bf16 v[124:127], v[188:191], a[60:63], v[124:127]\n"
    "ds_read_b128 a[92:95], v211 offset:38912\n"
    "s_waitcnt lgkmcnt(4)\n"
    "v_mfma_f32_16x16x32_bf16 v[0:3], v[128:131], a[64:67], v[0:3]\n"
    "ds_read_b64_tr_b16 v[136:137], v205 offset:49152\n"
    "v_mfma_f32_16x16x32_bf16 v[0:3], v[132:135], a[80:83], v[0:3]\n"
    "ds_read_b64_tr_b16 v[138:139], v205 offset:49664\n"
    "buffer_load_dwordx4 v197, s[28:31], s76 offen offset:3072 lds\n"
    "v_mfma_f32_16x16x32_bf16 v[4:7], v[128:131], a[68:71], v[4:7]\n"
    "ds_read_b64_tr_b16 v[140:141], v205 offset:57344\n"
    "s_add_u32 m0, s63, 65536\n"
    "v_mfma_f32_16x16x32_bf16 v[4:7], v[132:135], a[84:87], v[4:7]\n"
    "ds_read_b64_tr_b16 v[142:143], v205 offset:57856\n"
    "s_waitcnt lgkmcnt(4)\n"
    "v_mfma_f32_16x16x32_bf16 v[8:11], v[128:131], a[72:75], v[8:11]\n"
    "buffer_load_dwordx4 v198, s[32:35], s82 offen offset:0 lds\n"
    "ds_read_b64_tr_b16 v[144:145], v204 offset:51200\n"
    "v_mfma_f32_16x16x32_bf16 v[8:11], v[132:135], a[88:91], v[8:11]\n"
    "ds_read_b64_tr_b16 v[146:147], v204 offset:51712\n"
    "v_mfma_f32_16x16x32_bf16 v[12:15], v[128:131], a[76:79], v[12:15]\n"
    "buffer_load_dwordx4 v199, s[32:35], s82 offen offset:1024 lds\n"
    "ds_read_b64_tr_b16 v[148:149], v204 offset:59392\n"
    "v_mfma_f32_16x16x32_bf16 v[12:15], v[132:135], a[92:95], v[12:15]\n"
    "ds_read_b64_tr_b16 v[150:151], v204 offset:59904\n"
    "s_waitcnt lgkmcnt(4)\n"
    "v_mfma_f32_16x16x32_bf16 v[16:19], v[136:139], a[64:67], v[16:19]\n"
    "buffer_load_dwordx4 v200, s[32:35], s82 offen offset:2048 lds\n"
    "ds_read_b64_tr_b16 v[152:153], v205 offset:51200\n"
    "v_mfma_f32_16x16x32_bf16 v[16:19], v[140:143], a[80:83], v[16:19]\n"
    "ds_read_b64_tr_b16 v[154:155], v205 offset:51712\n"
    "v_mfma_f32_16x16x32_bf16 v[20:23], v[136:139], a[68:71], v[20:23]\n"
    "ds_read_b64_tr_b16 v[156:157], v205 offset:59392\n"
    "v_mfma_f32_16x16x32_bf16 v[20:23], v[140:143], a[84:87], v[20:23]\n"
    "buffer_load_dwordx4 v201, s[32:35], s82 offen offset:3072 lds\n"
    "ds_read_b64_tr_b16 v[158:159], v205 offset:59904\n"
    "v_mfma_f32_16x16x32_bf16 v[24:27], v[136:139], a[72:75], v[24:27]\n"
    "ds_read_b64_tr_b16 a[224:225], v214 offset:16384\n"
    "s_add_u32 m0, s63, 81920\n"
    "v_mfma_f32_16x16x32_bf16 v[24:27], v[140:143], a[88:91], v[24:27]\n"
    "ds_read_b64_tr_b16 a[226:227], v214 offset:18432\n"
    "v_mfma_f32_16x16x32_bf16 v[28:31], v[136:139], a[76:79], v[28:31]\n"
    "buffer_load_dwordx4 v192, s[20:23], s69 offen offset:0 lds\n"
    "ds_read_b64_tr_b16 a[228:229], v215 offset:16384\n"
    "v_mfma_f32_16x16x32_bf16 v[28:31], v[140:143], a[92:95], v[28:31]\n"
    "ds_read_b64_tr_b16 a[230:231], v215 offset:18432\n"
    "s_waitcnt lgkmcnt(4)\n"
    "v_mfma_f32_16x16x32_bf16 v[32:35], v[144:147], a[64:67], v[32:35]\n"
    "ds_read_b64_tr_b16 a[232:233], v216 offset:16384\n"
    "v_mfma_f32_16x16x32_bf16 v[32:35], v[148:151], a[80:83], v[32:35]\n"
    "buffer_load_dwordx4 v193, s[20:23], s69 offen offset:1024 lds\n"
    "ds_read_b64_tr_b16 a[234:235], v216 offset:18432\n"
    "v_mfma_f32_16x16x32_bf16 v[36:39], v[144:147], a[68:71], v[36:39]\n"
    "ds_read_b64_tr_b16 a[236:237], v217 offset:16384\n"
    "v_mfma_f32_16x16x32_bf16 v[36:39], v[148:151], a[84:87], v[36:39]\n"
    "ds_read_b64_tr_b16 a[238:239], v217 offset:18432\n"
    "v_mfma_f32_16x16x32_bf16 v[40:43], v[144:147], a[72:75], v[40:43]\n"
    "s_waitcnt vmcnt(12)\n"
    "s_barrier\n"
    "ds_read_b64_tr_b16 v[160:161], v202 offset:32768\n"
    "v_mfma_f32_16x16x32_bf16 v[40:43], v[148:151], a[88:91], v[40:43]\n"
    "buffer_load_dwordx4 v194, s[20:23], s69 offen offset:2048 lds\n"
    "ds_read_b64_tr_b16 v[162:163], v202 offset:33280\n"
    "v_mfma_f32_16x16x32_bf16 v[44:47], v[144:147], a[76:79], v[44:47]\n"
    "ds_read_b128 a[96:99], v212 offset:0\n"
    "v_mfma_f32_16x16x32_bf16 v[44:47], v[148:151], a[92:95], v[44:47]\n"
    "buffer_load_dwordx4 v195, s[20:23], s69 offen offset:3072 lds\n"
    "ds_read_b64_tr_b16 v[164:165], v202 offset:40960\n"
    "v_mfma_f32_16x16x32_bf16 v[48:51], v[152:155], a[64:67], v[48:51]\n"
    "ds_read_b64_tr_b16 v[166:167], v202 offset:41472\n"
    "v_mfma_f32_16x16x32_bf16 v[48:51], v[156:159], a[80:83], v[48:51]\n"
    "ds_read_b128 a[112:115], v213 offset:0\n"
    "s_add_u32 m0, s63, 0\n"
    "v_mfma_f32_16x16x32_bf16 v[52:55], v[152:155], a[68:71], v[52:55]\n"
    "ds_read_b128 a[100:103], v212 offset:2048\n"
    "v_mfma_f32_16x16x32_bf16 v[52:55], v[156:159], a[84:87], v[52:55]\n"
    "ds_read_b128 a[116:119], v213 offset:2048\n"
    "v_mfma_f32_16x16x32_bf16 v[56:59], v[152:155], a[72:75], v[56:59]\n"
    "ds_read_b128 a[104:107], v212 offset:4096\n"
    "v_mfma_f32_16x16x32_bf16 v[56:59], v[156:159], a[88:91], v[56:59]\n"
    "ds_read_b128 a[120:123], v213 offset:4096\n"
    "v_mfma_f32_16x16x32_bf16 v[60:63], v[152:155], a[76:79], v[60:63]\n"
    "ds_read_b128 a[108:111], v212 offset:6144\n"
    "buffer_load_dwordx4 v196, s[24:27], s77 offen offset:0 lds\n"
    "v_mfma_f32_16x16x32_bf16 v[60:63], v[156:159], a[92:95], v[60:63]\n"
    "ds_read_b128 a[124:127], v213 offset:6144\n"
    "s_waitcnt lgkmcnt(4)\n"
    "v_mfma_f32_16x16x32_bf16 v[0:3], v[160:163], a[96:99], v[0:3]\n"
    "ds_read_b64_tr_b16 v[168:169], v203 offset:32768\n"
    "v_mfma_f32_16x16x32_bf16 v[0:3], v[164:167], a[112:115], v[0:3]\n"
    "ds_read_b64_tr_b16 v[170:171], v203 offset:33280\n"
    "buffer_load_dwordx4 v197, s[24:27], s77 offen offset:1024 lds\n"
    "v_mfma_f32_16x16x32_bf16 v[4:7], v[160:163], a[100:103], v[4:7]\n"
    "ds_read_b64_tr_b16 v[172:173], v203 offset:40960\n"
    "v_mfma_f32_16x16x32_bf16 v[4:7], v[164:167], a[116:119], v[4:7]\n"
    "ds_read_b64_tr_b16 v[174:175], v203 offset:41472\n"
    "s_waitcnt lgkmcnt(4)\n"
    "v_mfma_f32_16x16x32_bf16 v[8:11], v[160:163], a[104:107], v[8:11]\n"
    "buffer_load_dwordx4 v196, s[28:31], s77 offen offset:2048 lds\n"
    "ds_read_b64_tr_b16 v[176:177], v202 offset:34816\n"
    "v_mfma_f32_16x16x32_bf16 v[8:11], v[164:167], a[120:123], v[8:11]\n"
    "ds_read_b64_tr_b16 v[178:179], v202 offset:35328\n"
    "v_mfma_f32_16x16x32_bf16 v[12:15], v[160:163], a[108:111], v[12:15]\n"
    "buffer_load_dwordx4 v197, s[28:31], s77 offen offset:3072 lds\n"
    "ds_read_b64_tr_b16 v[180:181], v202 offset:43008\n"
    "v_mfma_f32_16x16x32_bf16 v[12:15], v[164:167], a[124:127], v[12:15]\n"
    "ds_read_b64_tr_b16 v[182:183], v202 offset:43520\n"
    "s_add_u32 m0, s63, 98304\n"
    "s_waitcnt lgkmcnt(4)\n"
    "v_mfma_f32_16x16x32_bf16 v[16:19], v[168:171], a[96:99], v[16:19]\n"
    "buffer_load_dwordx4 v192, s[20:23], s70 offen offset:0 lds\n"
    "ds_read_b64_tr_b16 v[184:185], v203 offset:34816\n"
    "v_mfma_f32_16x16x32_bf16 v[16:19], v[172:175], a[112:115], v[16:19]\n"
    "ds_read_b64_tr_b16 v[186:187], v203 offset:35328\n"
    "v_mfma_f32_16x16x32_bf16 v[20:23], v[168:171], a[100:103], v[20:23]\n"
    "ds_read_b64_tr_b16 v[188:189], v203 offset:43008\n"
    "v_mfma_f32_16x16x32_bf16 v[20:23], v[172:175], a[116:119], v[20:23]\n"
    "buffer_load_dwordx4 v193, s[20:23], s70 offen offset:1024 lds\n"
    "ds_read_b64_tr_b16 v[190:191], v203 offset:43520\n"
    "v_mfma_f32_16x16x32_bf16 v[24:27], v[168:171], a[104:107], v[24:27]\n"
    "ds_read_b64_tr_b16 a[240:241], v214 offset:20480\n"
    "v_mfma_f32_16x16x32_bf16 v[24:27], v[172:175], a[120:123], v[24:27]\n"
    "ds_read_b64_tr_b16 a[242:243], v214 offset:22528\n"
    "v_mfma_f32_16x16x32_bf16 v[28:31], v[168:171], a[108:111], v[28:31]\n"
    "buffer_load_dwordx4 v194, s[20:23], s70 offen offset:2048 lds\n"
    "ds_read_b64_tr_b16 a[244:245], v215 offset:20480\n"
    "v_mfma_f32_16x16x32_bf16 v[28:31], v[172:175], a[124:127], v[28:31]\n"
    "ds_read_b64_tr_b16 a[246:247], v215 offset:22528\n"
    "s_waitcnt lgkmcnt(4)\n"
    "v_mfma_f32_16x16x32_bf16 v[32:35], v[176:179], a[96:99], v[32:35]\n"
    "ds_read_b64_tr_b16 a[248:249], v216 offset:20480\n"
    "v_mfma_f32_16x16x32_bf16 v[32:35], v[180:183], a[112:115], v[32:35]\n"
    "buffer_load_dwordx4 v195, s[20:23], s70 offen offset:3072 lds\n"
    "ds_read_b64_tr_b16 a[250:251], v216 offset:22528\n"
    "v_mfma_f32_16x16x32_bf16 v[36:39], v[176:179], a[100:103], v[36:39]\n"
    "ds_read_b64_tr_b16 a[252:253], v217 offset:20480\n"
    "s_add_u32 m0, s63, 114688\n"
    "v_mfma_f32_16x16x32_bf16 v[36:39], v[180:183], a[116:119], v[36:39]\n"
    "ds_read_b64_tr_b16 a[254:255], v217 offset:22528\n"
    "v_mfma_f32_16x16x32_bf16 v[40:43], v[176:179], a[104:107], v[40:43]\n"
    "s_waitcnt vmcnt(12)\n"
    "s_barrier\n"
    "v_mfma_f32_16x16x32_bf16 v[40:43], v[180:183], a[120:123], v[40:43]\n"
    "buffer_load_dwordx4 v196, s[24:27], s78 offen offset:0 lds\n"
    "v_mfma_f32_16x16x32_bf16 v[44:47], v[176:179], a[108:111], v[44:47]\n"
    "ds_read_b64_tr_b16 v[128:129], v206 offset:16384\n"
    "v_mfma_f32_16x16x32_bf16 v[44:47], v[180:183], a[124:127], v[44:47]\n"
    "buffer_load_dwordx4 v197, s[24:27], s78 offen offset:1024 lds\n"
    "v_mfma_f32_16x16x32_bf16 v[48:51], v[184:187], a[96:99], v[48:51]\n"
    "ds_read_b64_tr_b16 v[130:131], v206 offset:16896\n"
    "v_mfma_f32_16x16x32_bf16 v[48:51], v[188:191], a[112:115], v[48:51]\n"
    "ds_read_b128 a[128:131], v208 offset:49152\n"
    "v_mfma_f32_16x16x32_bf16 v[52:55], v[184:187], a[100:103], v[52:55]\n"
    "ds_read_b64_tr_b16 v[132:133], v206 offset:24576\n"
    "v_mfma_f32_16x16x32_bf16 v[52:55], v[188:191], a[116:119], v[52:55]\n"
    "ds_read_b64_tr_b16 v[134:135], v206 offset:25088\n"
    "v_mfma_f32_16x16x32_bf16 v[56:59], v[184:187], a[104:107], v[56:59]\n"
    "ds_read_b128 a[144:147], v209 offset:49152\n"
    "v_mfma_f32_16x16x32_bf16 v[56:59], v[188:191], a[120:123], v[56:59]\n"
    "ds_read_b128 a[132:135], v208 offset:51200\n"
    "v_mfma_f32_16x16x32_bf16 v[60:63], v[184:187], a[108:111], v[60:63]\n"
    "ds_read_b128 a[148:151], v209 offset:51200\n"
    "buffer_load_dwordx4 v196, s[28:31], s78 offen offset:2048 lds\n"
    "v_mfma_f32_16x16x32_bf16 v[60:63], v[188:191], a[124:127], v[60:63]\n"
    "ds_read_b128 a[136:139], v208 offset:53248\n"
    "s_waitcnt lgkmcnt(4)\n"
    "v_mfma_f32_16x16x32_bf16 v[0:3], v[128:131], a[128:131], v[0:3]\n"
    "ds_read_b128 a[152:155], v209 offset:53248\n"
    "s_waitcnt lgkmcnt(4)\n"
    "v_mfma_f32_16x16x32_bf16 v[0:3], v[132:135], a[144:147], v[0:3]\n"
    "ds_read_b128 a[140:143], v208 offset:55296\n"
    "buffer_load_dwordx4 v197, s[28:31], s78 offen offset:3072 lds\n"
    "s_waitcnt lgkmcnt(4)\n"
    "v_mfma_f32_16x16x32_bf16 v[4:7], v[128:131], a[132:135], v[4:7]\n"
    "ds_read_b128 a[156:159], v209 offset:55296\n"
    "s_add_u32 m0, s63, 32768\n"
    "s_waitcnt lgkmcnt(4)\n"
    "v_mfma_f32_16x16x32_bf16 v[4:7], v[132:135], a[148:151], v[4:7]\n"
    "ds_read_b64_tr_b16 v[136:137], v207 offset:16384\n"
    "ds_read_b64_tr_b16 v[138:139], v207 offset:16896\n"
    "s_waitcnt lgkmcnt(5)\n"
    "v_mfma_f32_16x16x32_bf16 v[8:11], v[128:131], a[136:139], v[8:11]\n"
    "ds_read_b64_tr_b16 v[140:141], v207 offset:24576\n"
    "ds_read_b64_tr_b16 v[142:143], v207 offset:25088\n"
    "s_waitcnt lgkmcnt(6)\n"
    "v_mfma_f32_16x16x32_bf16 v[8:11], v[132:135], a[152:155], v[8:11]\n"
    "buffer_load_dwordx4 v192, s[20:23], s71 offen offset:0 lds\n"
    "s_waitcnt lgkmcnt(5)\n"
    "v_mfma_f32_16x16x32_bf16 v[12:15], v[128:131], a[140:143], v[12:15]\n"
    "ds_read_b64_tr_b16 v[144:145], v206 offset:18432\n"
    "s_waitcnt lgkmcnt(5)\n"
    "v_mfma_f32_16x16x32_bf16 v[12:15], v[132:135], a[156:159], v[12:15]\n"
    "buffer_load_dwordx4 v193, s[20:23], s71 offen offset:1024 lds\n"
    "s_waitcnt lgkmcnt(3)\n"
    "v_mfma_f32_16x16x32_bf16 v[16:19], v[136:139], a[128:131], v[16:19]\n"
    "ds_read_b64_tr_b16 v[146:147], v206 offset:18944\n"
    "s_waitcnt lgkmcnt(2)\n"
    "v_mfma_f32_16x16x32_bf16 v[16:19], v[140:143], a[144:147], v[16:19]\n"
    "ds_read_b64_tr_b16 v[148:149], v206 offset:26624\n"
    "v_mfma_f32_16x16x32_bf16 v[20:23], v[136:139], a[132:135], v[20:23]\n"
    "ds_read_b64_tr_b16 v[150:151], v206 offset:27136\n"
    "v_mfma_f32_16x16x32_bf16 v[20:23], v[140:143], a[148:151], v[20:23]\n"
    "buffer_load_dwordx4 v194, s[20:23], s71 offen offset:2048 lds\n"
    "v_mfma_f32_16x16x32_bf16 v[24:27], v[136:139], a[136:139], v[24:27]\n"
    "ds_read_b64_tr_b16 v[152:153], v207 offset:18432\n"
    "v_mfma_f32_16x16x32_bf16 v[24:27], v[140:143], a[152:155], v[24:27]\n"
    "buffer_load_dwordx4 v195, s[20:23], s71 offen offset:3072 lds\n"
    "v_mfma_f32_16x16x32_bf16 v[28:31], v[136:139], a[140:143], v[28:31]\n"
    "ds_read_b64_tr_b16 v[154:155], v207 offset:18944\n"
    "v_mfma_f32_16x16x32_bf16 v[28:31], v[140:143], a[156:159], v[28:31]\n"
    "ds_read_b64_tr_b16 v[156:157], v207 offset:26624\n"
    "s_waitcnt lgkmcnt(3)\n"
    "v_mfma_f32_16x16x32_bf16 v[32:35], v[144:147], a[128:131], v[32:35]\n"
    "ds_read_b64_tr_b16 v[158:159], v207 offset:27136\n"
    "s_add_u32 m0, s63, 131072\n"
    "v_mfma_f32_16x16x32_bf16 v[32:35], v[148:151], a[144:147], v[32:35]\n"
    "buffer_load_dwordx4 v196, s[24:27], s79 offen offset:0 lds\n"
    "v_mfma_f32_16x16x32_bf16 v[36:39], v[144:147], a[132:135], v[36:39]\n"
    "v_mfma_f32_16x16x32_bf16 v[36:39], v[148:151], a[148:151], v[36:39]\n"
    "v_mfma_f32_16x16x32_bf16 v[40:43], v[144:147], a[136:139], v[40:43]\n"
    "s_waitcnt vmcnt(12)\n"
    "s_barrier\n"
    "v_mfma_f32_16x16x32_bf16 v[40:43], v[148:151], a[152:155], v[40:43]\n"
    "buffer_load_dwordx4 v197, s[24:27], s79 offen offset:1024 lds\n"
    "v_mfma_f32_16x16x32_bf16 v[44:47], v[144:147], a[140:143], v[44:47]\n"
    "ds_read_b64_tr_b16 v[160:161], v202 offset:0\n"
    "v_mfma_f32_16x16x32_bf16 v[44:47], v[148:151], a[156:159], v[44:47]\n"
    "ds_read_b64_tr_b16 v[162:163], v202 offset:512\n"
    "s_waitcnt lgkmcnt(2)\n"
    "v_mfma_f32_16x16x32_bf16 v[48:51], v[152:155], a[128:131], v[48:51]\n"
    "ds_read_b128 a[160:163], v210 offset:16384\n"
    "v_mfma_f32_16x16x32_bf16 v[48:51], v[156:159], a[144:147], v[48:51]\n"
    "ds_read_b64_tr_b16 v[164:165], v202 offset:8192\n"
    "v_mfma_f32_16x16x32_bf16 v[52:55], v[152:155], a[132:135], v[52:55]\n"
    "ds_read_b64_tr_b16 v[166:167], v202 offset:8704\n"
    "v_mfma_f32_16x16x32_bf16 v[52:55], v[156:159], a[148:151], v[52:55]\n"
    "ds_read_b128 a[176:179], v211 offset:16384\n"
    "v_mfma_f32_16x16x32_bf16 v[56:59], v[152:155], a[136:139], v[56:59]\n"
    "ds_read_b128 a[164:167], v210 offset:18432\n"
    "v_mfma_f32_16x16x32_bf16 v[56:59], v[156:159], a[152:155], v[56:59]\n"
    "ds_read_b128 a[180:183], v211 offset:18432\n"
    "buffer_load_dwordx4 v196, s[28:31], s79 offen offset:2048 lds\n"
    "v_mfma_f32_16x16x32_bf16 v[60:63], v[152:155], a[140:143], v[60:63]\n"
    "ds_read_b128 a[168:171], v210 offset:20480\n"
    "v_mfma_f32_16x16x32_bf16 v[60:63], v[156:159], a[156:159], v[60:63]\n"
    "ds_read_b128 a[184:187], v211 offset:20480\n"
    "s_waitcnt lgkmcnt(4)\n"
    "v_mfma_f32_16x16x32_bf16 v[0:3], v[160:163], a[160:163], v[0:3]\n"
    "ds_read_b128 a[172:175], v210 offset:22528\n"
    "v_mfma_f32_16x16x32_bf16 v[0:3], v[164:167], a[176:179], v[0:3]\n"
    "ds_read_b128 a[188:191], v211 offset:22528\n"
    "buffer_load_dwordx4 v197, s[28:31], s79 offen offset:3072 lds\n"
    "s_waitcnt lgkmcnt(4)\n"
    "v_mfma_f32_16x16x32_bf16 v[4:7], v[160:163], a[164:167], v[4:7]\n"
    "ds_read_b64_tr_b16 v[168:169], v203 offset:0\n"
    "s_add_u32 s98, s74, 128\n"
    "s_add_u32 m0, s63, 16384\n"
    "v_mfma_f32_16x16x32_bf16 v[4:7], v[164:167], a[180:183], v[4:7]\n"
    "ds_read_b64_tr_b16 v[170:171], v203 offset:512\n"
    "s_waitcnt lgkmcnt(4)\n"
    "v_mfma_f32_16x16x32_bf16 v[8:11], v[160:163], a[168:171], v[8:11]\n"
    "ds_read_b64_tr_b16 v[172:173], v203 offset:8192\n"
    "ds_read_b64_tr_b16 v[174:175], v203 offset:8704\n"
    "v_mfma_f32_16x16x32_bf16 v[8:11], v[164:167], a[184:187], v[8:11]\n"
    "buffer_load_dwordx4 v196, s[24:27], s98 offen offset:0 lds\n"
    "s_waitcnt lgkmcnt(4)\n"
    "v_mfma_f32_16x16x32_bf16 v[12:15], v[160:163], a[172:175], v[12:15]\n"
    "ds_read_b64_tr_b16 v[176:177], v202 offset:2048\n"
    "v_mfma_f32_16x16x32_bf16 v[12:15], v[164:167], a[188:191], v[12:15]\n"
    "buffer_load_dwordx4 v197, s[24:27], s98 offen offset:1024 lds\n"
    "s_waitcnt lgkmcnt(3)\n"
    "v_mfma_f32_16x16x32_bf16 v[16:19], v[168:171], a[160:163], v[16:19]\n"
    "ds_read_b64_tr_b16 v[178:179], v202 offset:2560\n"
    "s_waitcnt lgkmcnt(2)\n"
    "v_mfma_f32_16x16x32_bf16 v[16:19], v[172:175], a[176:179], v[16:19]\n"
    "ds_read_b64_tr_b16 v[180:181], v202 offset:10240\n"
    "v_mfma_f32_16x16x32_bf16 v[20:23], v[168:171], a[164:167], v[20:23]\n"
    "ds_read_b64_tr_b16 v[182:183], v202 offset:10752\n"
    "v_mfma_f32_16x16x32_bf16 v[20:23], v[172:175], a[180:183], v[20:23]\n"
    "buffer_load_dwordx4 v196, s[28:31], s98 offen offset:2048 lds\n"
    "v_mfma_f32_16x16x32_bf16 v[24:27], v[168:171], a[168:171], v[24:27]\n"
    "ds_read_b64_tr_b16 v[184:185], v203 offset:2048\n"
    "v_mfma_f32_16x16x32_bf16 v[24:27], v[172:175], a[184:187], v[24:27]\n"
    "buffer_load_dwordx4 v197, s[28:31], s98 offen offset:3072 lds\n"
    "v_mfma_f32_16x16x32_bf16 v[28:31], v[168:171], a[172:175], v[28:31]\n"
    "ds_read_b64_tr_b16 v[186:187], v203 offset:2560\n"
    "v_mfma_f32_16x16x32_bf16 v[28:31], v[172:175], a[188:191], v[28:31]\n"
    "ds_read_b64_tr_b16 v[188:189], v203 offset:10240\n"
    "s_waitcnt lgkmcnt(3)\n"
    "v_mfma_f32_16x16x32_bf16 v[32:35], v[176:179], a[160:163], v[32:35]\n"
    "ds_read_b64_tr_b16 v[190:191], v203 offset:10752\n"
    "s_add_u32 m0, s63, 49152\n"
    "v_mfma_f32_16x16x32_bf16 v[32:35], v[180:183], a[176:179], v[32:35]\n"
    "buffer_load_dwordx4 v198, s[32:35], s83 offen offset:0 lds\n"
    "v_mfma_f32_16x16x32_bf16 v[36:39], v[176:179], a[164:167], v[36:39]\n"
    "v_mfma_f32_16x16x32_bf16 v[36:39], v[180:183], a[180:183], v[36:39]\n"
    "v_mfma_f32_16x16x32_bf16 v[40:43], v[176:179], a[168:171], v[40:43]\n"
    "s_waitcnt vmcnt(12)\n"
    "s_barrier\n"
    "v_mfma_f32_16x16x32_bf16 v[40:43], v[180:183], a[184:187], v[40:43]\n"
    "buffer_load_dwordx4 v199, s[32:35], s83 offen offset:1024 lds\n"
    "v_mfma_f32_16x16x32_bf16 v[44:47], v[176:179], a[172:175], v[44:47]\n"
    "ds_read_b64_tr_b16 v[128:129], v204 offset:49152\n"
    "v_mfma_f32_16x16x32_bf16 v[44:47], v[180:183], a[188:191], v[44:47]\n"
    "ds_read_b64_tr_b16 v[130:131], v204 offset:49664\n"
    "s_waitcnt lgkmcnt(2)\n"
    "v_mfma_f32_16x16x32_bf16 v[48:51], v[184:187], a[160:163], v[48:51]\n"
    "ds_read_b128 a[0:3], v210 offset:32768\n"
    "v_mfma_f32_16x16x32_bf16 v[48:51], v[188:191], a[176:179], v[48:51]\n"
    "ds_read_b64_tr_b16 v[132:133], v204 offset:57344\n"
    "v_mfma_f32_16x16x32_bf16 v[52:55], v[184:187], a[164:167], v[52:55]\n"
    "ds_read_b64_tr_b16 v[134:135], v204 offset:57856\n"
    "v_mfma_f32_16x16x32_bf16 v[52:55], v[188:191], a[180:183], v[52:55]\n"
    "ds_read_b128 a[16:19], v211 offset:32768\n"
    "v_mfma_f32_16x16x32_bf16 v[56:59], v[184:187], a[168:171], v[56:59]\n"
    "ds_read_b128 a[4:7], v210 offset:34816\n"
    "v_mfma_f32_16x16x32_bf16 v[56:59], v[188:191], a[184:187], v[56:59]\n"
    "ds_read_b128 a[20:23], v211 offset:34816\n"
    "buffer_load_dwordx4 v200, s[32:35], s83 offen offset:2048 lds\n"
    "v_mfma_f32_16x16x32_bf16 v[60:63], v[184:187], a[172:175], v[60:63]\n"
    "ds_read_b128 a[8:11], v210 offset:36864\n"
    "v_mfma_f32_16x16x32_bf16 v[60:63], v[188:191], a[188:191], v[60:63]\n"
    "ds_read_b128 a[24:27], v211 offset:36864\n"
    "s_waitcnt lgkmcnt(4)\n"
    "v_mfma_f32_16x16x32_bf16 v[0:3], v[128:131], a[0:3], v[0:3]\n"
    "ds_read_b128 a[12:15], v210 offset:38912\n"
    "v_mfma_f32_16x16x32_bf16 v[0:3], v[132:135], a[16:19], v[0:3]\n"
    "ds_read_b128 a[28:31], v211 offset:38912\n"
    "buffer_load_dwordx4 v201, s[32:35], s83 offen offset:3072 lds\n"
    "s_waitcnt lgkmcnt(4)\n"
    "v_mfma_f32_16x16x32_bf16 v[4:7], v[128:131], a[4:7], v[4:7]\n"
    "ds_read_b64_tr_b16 v[136:137], v205 offset:49152\n"
    "s_add_u32 s99, s75, 128\n"
    "s_add_u32 m0, s63, 81920\n"
    "v_mfma_f32_16x16x32_bf16 v[4:7], v[132:135], a[20:23], v[4:7]\n"
    "ds_read_b64_tr_b16 v[138:139], v205 offset:49664\n"
    "s_waitcnt lgkmcnt(4)\n"
    "v_mfma_f32_16x16x32_bf16 v[8:11], v[128:131], a[8:11], v[8:11]\n"
    "ds_read_b64_tr_b16 v[140:141], v205 offset:57344\n"
    "ds_read_b64_tr_b16 v[142:143], v205 offset:57856\n"
    "v_mfma_f32_16x16x32_bf16 v[8:11], v[132:135], a[24:27], v[8:11]\n"
    "buffer_load_dwordx4 v196, s[24:27], s99 offen offset:0 lds\n"
    "s_waitcnt lgkmcnt(4)\n"
    "v_mfma_f32_16x16x32_bf16 v[12:15], v[128:131], a[12:15], v[12:15]\n"
    "ds_read_b64_tr_b16 v[144:145], v204 offset:51200\n"
    "v_mfma_f32_16x16x32_bf16 v[12:15], v[132:135], a[28:31], v[12:15]\n"
    "buffer_load_dwordx4 v197, s[24:27], s99 offen offset:1024 lds\n"
    "s_waitcnt lgkmcnt(3)\n"
    "v_mfma_f32_16x16x32_bf16 v[16:19], v[136:139], a[0:3], v[16:19]\n"
    "ds_read_b64_tr_b16 v[146:147], v204 offset:51712\n"
    "s_waitcnt lgkmcnt(2)\n"
    "v_mfma_f32_16x16x32_bf16 v[16:19], v[140:143], a[16:19], v[16:19]\n"
    "ds_read_b64_tr_b16 v[148:149], v204 offset:59392\n"
    "v_mfma_f32_16x16x32_bf16 v[20:23], v[136:139], a[4:7], v[20:23]\n"
    "ds_read_b64_tr_b16 v[150:151], v204 offset:59904\n"
    "v_mfma_f32_16x16x32_bf16 v[20:23], v[140:143], a[20:23], v[20:23]\n"
    "buffer_load_dwordx4 v196, s[28:31], s99 offen offset:2048 lds\n"
    "v_mfma_f32_16x16x32_bf16 v[24:27], v[136:139], a[8:11], v[24:27]\n"
    "ds_read_b64_tr_b16 v[152:153], v205 offset:51200\n"
    "v_mfma_f32_16x16x32_bf16 v[24:27], v[140:143], a[24:27], v[24:27]\n"
    "buffer_load_dwordx4 v197, s[28:31], s99 offen offset:3072 lds\n"
    "v_mfma_f32_16x16x32_bf16 v[28:31], v[136:139], a[12:15], v[28:31]\n"
    "ds_read_b64_tr_b16 v[154:155], v205 offset:51712\n"
    "v_mfma_f32_16x16x32_bf16 v[28:31], v[140:143], a[28:31], v[28:31]\n"
    "ds_read_b64_tr_b16 v[156:157], v205 offset:59392\n"
    "s_waitcnt lgkmcnt(3)\n"
    "v_mfma_f32_16x16x32_bf16 v[32:35], v[144:147], a[0:3], v[32:35]\n"
    "ds_read_b64_tr_b16 v[158:159], v205 offset:59904\n"
    "s_add_u32 m0, s63, 147456\n"
    "v_mfma_f32_16x16x32_bf16 v[32:35], v[148:151], a[16:19], v[32:35]\n"
    "buffer_load_dwordx4 v198, s[32:35], s84 offen offset:0 lds\n"
    "v_mfma_f32_16x16x32_bf16 v[36:39], v[144:147], a[4:7], v[36:39]\n"
    "v_mfma_f32_16x16x32_bf16 v[36:39], v[148:151], a[20:23], v[36:39]\n"
    "v_mfma_f32_16x16x32_bf16 v[40:43], v[144:147], a[8:11], v[40:43]\n"
    "s_waitcnt vmcnt(12)\n"
    "s_barrier\n"
    "v_mfma_f32_16x16x32_bf16 v[40:43], v[148:151], a[24:27], v[40:43]\n"
    "buffer_load_dwordx4 v199, s[32:35], s84 offen offset:1024 lds\n"
    "v_mfma_f32_16x16x32_bf16 v[44:47], v[144:147], a[12:15], v[44:47]\n"
    "ds_read_b64_tr_b16 v[160:161], v206 offset:0\n"
    "v_mfma_f32_16x16x32_bf16 v[44:47], v[148:151], a[28:31], v[44:47]\n"
    "ds_read_b64_tr_b16 v[162:163], v206 offset:512\n"
    "s_waitcnt lgkmcnt(2)\n"
    "v_mfma_f32_16x16x32_bf16 v[48:51], v[152:155], a[0:3], v[48:51]\n"
    "ds_read_b128 a[32:35], v208 offset:32768\n"
    "v_mfma_f32_16x16x32_bf16 v[48:51], v[156:159], a[16:19], v[48:51]\n"
    "ds_read_b64_tr_b16 v[164:165], v206 offset:8192\n"
    "v_mfma_f32_16x16x32_bf16 v[52:55], v[152:155], a[4:7], v[52:55]\n"
    "ds_read_b64_tr_b16 v[166:167], v206 offset:8704\n"
    "v_mfma_f32_16x16x32_bf16 v[52:55], v[156:159], a[20:23], v[52:55]\n"
    "ds_read_b128 a[48:51], v209 offset:32768\n"
    "v_mfma_f32_16x16x32_bf16 v[56:59], v[152:155], a[8:11], v[56:59]\n"
    "ds_read_b128 a[36:39], v208 offset:34816\n"
    "v_mfma_f32_16x16x32_bf16 v[56:59], v[156:159], a[24:27], v[56:59]\n"
    "ds_read_b128 a[52:55], v209 offset:34816\n"
    "buffer_load_dwordx4 v200, s[32:35], s84 offen offset:2048 lds\n"
    "v_mfma_f32_16x16x32_bf16 v[60:63], v[152:155], a[12:15], v[60:63]\n"
    "ds_read_b128 a[40:43], v208 offset:36864\n"
    "v_mfma_f32_16x16x32_bf16 v[60:63], v[156:159], a[28:31], v[60:63]\n"
    "ds_read_b128 a[56:59], v209 offset:36864\n"
    "s_waitcnt lgkmcnt(4)\n"
    "v_mfma_f32_16x16x32_bf16 v[0:3], v[160:163], a[32:35], v[0:3]\n"
    "ds_read_b128 a[44:47], v208 offset:38912\n"
    "v_mfma_f32_16x16x32_bf16 v[0:3], v[164:167], a[48:51], v[0:3]\n"
    "ds_read_b128 a[60:63], v209 offset:38912\n"
    "buffer_load_dwordx4 v201, s[32:35], s84 offen offset:3072 lds\n"
    "s_waitcnt lgkmcnt(4)\n"
    "v_mfma_f32_16x16x32_bf16 v[4:7], v[160:163], a[36:39], v[4:7]\n"
    "ds_read_b64_tr_b16 v[168:169], v207 offset:0\n"
    "s_add_u32 s98, s76, 128\n"
    "s_add_u32 m0, s63, 0\n"
    "v_mfma_f32_16x16x32_bf16 v[4:7], v[164:167], a[52:55], v[4:7]\n"
    "ds_read_b64_tr_b16 v[170:171], v207 offset:512\n"
    "s_waitcnt lgkmcnt(4)\n"
    "v_mfma_f32_16x16x32_bf16 v[8:11], v[160:163], a[40:43], v[8:11]\n"
    "ds_read_b64_tr_b16 v[172:173], v207 offset:8192\n"
    "ds_read_b64_tr_b16 v[174:175], v207 offset:8704\n"
    "v_mfma_f32_16x16x32_bf16 v[8:11], v[164:167], a[56:59], v[8:11]\n"
    "buffer_load_dwordx4 v196, s[24:27], s98 offen offset:0 lds\n"
    "s_waitcnt lgkmcnt(4)\n"
    "v_mfma_f32_16x16x32_bf16 v[12:15], v[160:163], a[44:47], v[12:15]\n"
    "ds_read_b64_tr_b16 v[176:177], v206 offset:2048\n"
    "v_mfma_f32_16x16x32_bf16 v[12:15], v[164:167], a[60:63], v[12:15]\n"
    "buffer_load_dwordx4 v197, s[24:27], s98 offen offset:1024 lds\n"
    "s_waitcnt lgkmcnt(3)\n"
    "v_mfma_f32_16x16x32_bf16 v[16:19], v[168:171], a[32:35], v[16:19]\n"
    "ds_read_b64_tr_b16 v[178:179], v206 offset:2560\n"
    "s_waitcnt lgkmcnt(2)\n"
    "v_mfma_f32_16x16x32_bf16 v[16:19], v[172:175], a[48:51], v[16:19]\n"
    "ds_read_b64_tr_b16 v[180:181], v206 offset:10240\n"
    "v_mfma_f32_16x16x32_bf16 v[20:23], v[168:171], a[36:39], v[20:23]\n"
    "ds_read_b64_tr_b16 v[182:183], v206 offset:10752\n"
    "v_mfma_f32_16x16x32_bf16 v[20:23], v[172:175], a[52:55], v[20:23]\n"
    "buffer_load_dwordx4 v196, s[28:31], s98 offen offset:2048 lds\n"
    "v_mfma_f32_16x16x32_bf16 v[24:27], v[168:171], a[40:43], v[24:27]\n"
    "ds_read_b64_tr_b16 v[184:185], v207 offset:2048\n"
    "v_mfma_f32_16x16x32_bf16 v[24:27], v[172:175], a[56:59], v[24:27]\n"
    "buffer_load_dwordx4 v197, s[28:31], s98 offen offset:3072 lds\n"
    "v_mfma_f32_16x16x32_bf16 v[28:31], v[168:171], a[44:47], v[28:31]\n"
    "ds_read_b64_tr_b16 v[186:187], v207 offset:2560\n"
    "v_mfma_f32_16x16x32_bf16 v[28:31], v[172:175], a[60:63], v[28:31]\n"
    "ds_read_b64_tr_b16 v[188:189], v207 offset:10240\n"
    "s_waitcnt lgkmcnt(3)\n"
    "v_mfma_f32_16x16x32_bf16 v[32:35], v[176:179], a[32:35], v[32:35]\n"
    "ds_read_b64_tr_b16 v[190:191], v207 offset:10752\n"
    "s_add_u32 m0, s63, 98304\n"
    "v_mfma_f32_16x16x32_bf16 v[32:35], v[180:183], a[48:51], v[32:35]\n"
    "buffer_load_dwordx4 v198, s[32:35], s85 offen offset:0 lds\n"
    "v_mfma_f32_16x16x32_bf16 v[36:39], v[176:179], a[36:39], v[36:39]\n"
    "v_mfma_f32_16x16x32_bf16 v[36:39], v[180:183], a[52:55], v[36:39]\n"
    "v_mfma_f32_16x16x32_bf16 v[40:43], v[176:179], a[40:43], v[40:43]\n"
    "s_waitcnt vmcnt(12)\n"
    "s_barrier\n"
    "v_mfma_f32_16x16x32_bf16 v[40:43], v[180:183], a[56:59], v[40:43]\n"
    "buffer_load_dwordx4 v199, s[32:35], s85 offen offset:1024 lds\n"
    "v_mfma_f32_16x16x32_bf16 v[44:47], v[176:179], a[44:47], v[44:47]\n"
    "ds_read_b64_tr_b16 v[128:129], v202 offset:16384\n"
    "v_mfma_f32_16x16x32_bf16 v[44:47], v[180:183], a[60:63], v[44:47]\n"
    "ds_read_b64_tr_b16 v[130:131], v202 offset:16896\n"
    "s_waitcnt lgkmcnt(2)\n"
    "v_mfma_f32_16x16x32_bf16 v[48:51], v[184:187], a[32:35], v[48:51]\n"
    "ds_read_b64_tr_b16 v[132:133], v202 offset:24576\n"
    "v_mfma_f32_16x16x32_bf16 v[48:51], v[188:191], a[48:51], v[48:51]\n"
    "ds_read_b64_tr_b16 v[134:135], v202 offset:25088\n"
    "v_mfma_f32_16x16x32_bf16 v[52:55], v[184:187], a[36:39], v[52:55]\n"
    "buffer_load_dwordx4 v200, s[32:35], s85 offen offset:2048 lds\n"
    "v_mfma_f32_16x16x32_bf16 v[52:55], v[188:191], a[52:55], v[52:55]\n"
    "ds_read_b64_tr_b16 v[136:137], v203 offset:16384\n"
    "v_mfma_f32_16x16x32_bf16 v[56:59], v[184:187], a[40:43], v[56:59]\n"
    "ds_read_b64_tr_b16 v[138:139], v203 offset:16896\n"
    "v_mfma_f32_16x16x32_bf16 v[56:59], v[188:191], a[56:59], v[56:59]\n"
    "buffer_load_dwordx4 v201, s[32:35], s85 offen offset:3072 lds\n"
    "v_mfma_f32_16x16x32_bf16 v[60:63], v[184:187], a[44:47], v[60:63]\n"
    "ds_read_b64_tr_b16 v[140:141], v203 offset:24576\n"
    "v_mfma_f32_16x16x32_bf16 v[60:63], v[188:191], a[60:63], v[60:63]\n"
    "ds_read_b64_tr_b16 v[142:143], v203 offset:25088\n"
    "s_waitcnt lgkmcnt(3)\n"
    "v_mfma_f32_16x16x32_bf16 v[64:67], v[128:131], a[64:67], v[64:67]\n"
    "ds_read_b64_tr_b16 v[144:145], v202 offset:18432\n"
    "s_add_u32 s99, s77, 128\n"
    "v_mfma_f32_16x16x32_bf16 v[64:67], v[132:135], a[80:83], v[64:67]\n"
    "ds_read_b64_tr_b16 v[146:147], v202 offset:18944\n"
    "s_add_u32 m0, s63, 32768\n"
    "v_mfma_f32_16x16x32_bf16 v[68:71], v[128:131], a[68:71], v[68:71]\n"
    "buffer_load_dwordx4 v196, s[24:27], s99 offen offset:0 lds\n"
    "v_mfma_f32_16x16x32_bf16 v[68:71], v[132:135], a[84:87], v[68:71]\n"
    "ds_read_b64_tr_b16 v[148:149], v202 offset:26624\n"
    "v_mfma_f32_16x16x32_bf16 v[72:75], v[128:131], a[72:75], v[72:75]\n"
    "ds_read_b64_tr_b16 v[150:151], v202 offset:27136\n"
    "v_mul_f32 v0, v0, v32\n"
    "v_mfma_f32_16x16x32_bf16 v[72:75], v[132:135], a[88:91], v[72:75]\n"
    "v_mul_f32 v1, v1, v33\n"
    "v_mul_f32 v2, v2, v34\n"
    "v_mfma_f32_16x16x32_bf16 v[76:79], v[128:131], a[76:79], v[76:79]\n"
    "buffer_load_dwordx4 v197, s[24:27], s99 offen offset:1024 lds\n"
    "v_mul_f32 v3, v3, v35\n"
    "v_mfma_f32_16x16x32_bf16 v[76:79], v[132:135], a[92:95], v[76:79]\n"
    "v_mul_f32 v16, v16, v48\n"
    "v_mul_f32 v17, v17, v49\n"
    "s_waitcnt lgkmcnt(1)\n"
    "v_mfma_f32_16x16x32_bf16 v[80:83], v[136:139], a[64:67], v[80:83]\n"
    "v_mul_f32 v18, v18, v50\n"
    "v_mul_f32 v19, v19, v51\n"
    "v_mfma_f32_16x16x32_bf16 v[80:83], v[140:143], a[80:83], v[80:83]\n"
    "v_cvt_pk_bf16_f32 v32, v0, v1\n"
    "v_cvt_pk_bf16_f32 v33, v2, v3\n"
    "v_mfma_f32_16x16x32_bf16 v[84:87], v[136:139], a[68:71], v[84:87]\n"
    "ds_read_b64_tr_b16 v[152:153], v203 offset:18432\n"
    "v_cvt_pk_bf16_f32 v34, v16, v17\n"
    "v_mfma_f32_16x16x32_bf16 v[84:87], v[140:143], a[84:87], v[84:87]\n"
    "buffer_load_dwordx4 v196, s[28:31], s99 offen offset:2048 lds\n"
    "v_cvt_pk_bf16_f32 v35, v18, v19\n"
    "v_mfma_f32_16x16x32_bf16 v[88:91], v[136:139], a[72:75], v[88:91]\n"
    "ds_read_b64_tr_b16 v[154:155], v203 offset:18944\n"
    "buffer_store_dwordx4 v[32:35], v246, s[36:39], s90 offen offset:0 sc1\n"
    "v_mfma_f32_16x16x32_bf16 v[88:91], v[140:143], a[88:91], v[88:91]\n"
    "ds_read_b64_tr_b16 v[156:157], v203 offset:26624\n"
    "v_mul_f32 v4, v4, v36\n"
    "v_mfma_f32_16x16x32_bf16 v[92:95], v[136:139], a[76:79], v[92:95]\n"
    "ds_read_b64_tr_b16 v[158:159], v203 offset:27136\n"
    "v_mul_f32 v5, v5, v37\n"
    "v_mfma_f32_16x16x32_bf16 v[92:95], v[140:143], a[92:95], v[92:95]\n"
    "buffer_load_dwordx4 v197, s[28:31], s99 offen offset:3072 lds\n"
    "v_mul_f32 v6, v6, v38\n"
    "v_mfma_f32_16x16x32_bf16 v[96:99], v[144:147], a[64:67], v[96:99]\n"
    "v_mul_f32 v7, v7, v39\n"
    "v_mul_f32 v20, v20, v52\n"
    "s_waitcnt lgkmcnt(2)\n"
    "v_mfma_f32_16x16x32_bf16 v[96:99], v[148:151], a[80:83], v[96:99]\n"
    "v_mul_f32 v21, v21, v53\n"
    "v_mul_f32 v22, v22, v54\n"
    "v_mfma_f32_16x16x32_bf16 v[100:103], v[144:147], a[68:71], v[100:103]\n"
    "s_add_u32 m0, s63, 114688\n"
    "v_mul_f32 v23, v23, v55\n"
    "v_cvt_pk_bf16_f32 v36, v4, v5\n"
    "v_mfma_f32_16x16x32_bf16 v[100:103], v[148:151], a[84:87], v[100:103]\n"
    "v_cvt_pk_bf16_f32 v37, v6, v7\n"
    "v_cvt_pk_bf16_f32 v38, v20, v21\n"
    "v_mfma_f32_16x16x32_bf16 v[104:107], v[144:147], a[72:75], v[104:107]\n"
    "s_waitcnt vmcnt(12)\n"
    "s_barrier\n"
    "v_cvt_pk_bf16_f32 v39, v22, v23\n"
    "v_mfma_f32_16x16x32_bf16 v[104:107], v[148:151], a[88:91], v[104:107]\n"
    "buffer_load_dwordx4 v198, s[32:35], s86 offen offset:0 lds\n"
    "v_mul_f32 v8, v8, v40\n"
    "v_mfma_f32_16x16x32_bf16 v[108:111], v[144:147], a[76:79], v[108:111]\n"
    "ds_read_b64_tr_b16 v[160:161], v204 offset:16384\n"
    "v_mul_f32 v9, v9, v41\n"
    "v_mfma_f32_16x16x32_bf16 v[108:111], v[148:151], a[92:95], v[108:111]\n"
    "ds_read_b64_tr_b16 v[162:163], v204 offset:16896\n"
    "v_mul_f32 v10, v10, v42\n"
    "v_mfma_f32_16x16x32_bf16 v[112:115], v[152:155], a[64:67], v[112:115]\n"
    "ds_read_b64_tr_b16 v[164:165], v204 offset:24576\n"
    "v_mul_f32 v11, v11, v43\n"
    "s_waitcnt lgkmcnt(3)\n"
    "v_mfma_f32_16x16x32_bf16 v[112:115], v[156:159], a[80:83], v[112:115]\n"
    "ds_read_b64_tr_b16 v[166:167], v204 offset:25088\n"
    "v_mul_f32 v24, v24, v56\n"
    "v_mfma_f32_16x16x32_bf16 v[116:119], v[152:155], a[68:71], v[116:119]\n"
    "buffer_load_dwordx4 v199, s[32:35], s86 offen offset:1024 lds\n"
    "v_mul_f32 v25, v25, v57\n"
    "v_mfma_f32_16x16x32_bf16 v[116:119], v[156:159], a[84:87], v[116:119]\n"
    "ds_read_b64_tr_b16 v[168:169], v205 offset:16384\n"
    "v_mul_f32 v26, v26, v58\n"
    "v_mfma_f32_16x16x32_bf16 v[120:123], v[152:155], a[72:75], v[120:123]\n"
    "ds_read_b64_tr_b16 v[170:171], v205 offset:16896\n"
    "v_mul_f32 v27, v27, v59\n"
    "v_mfma_f32_16x16x32_bf16 v[120:123], v[156:159], a[88:91], v[120:123]\n"
    "buffer_load_dwordx4 v200, s[32:35], s86 offen offset:2048 lds\n"
    "v_cvt_pk_bf16_f32 v40, v8, v9\n"
    "v_mfma_f32_16x16x32_bf16 v[124:127], v[152:155], a[76:79], v[124:127]\n"
    "ds_read_b64_tr_b16 v[172:173], v205 offset:24576\n"
    "v_cvt_pk_bf16_f32 v41, v10, v11\n"
    "v_mfma_f32_16x16x32_bf16 v[124:127], v[156:159], a[92:95], v[124:127]\n"
    "ds_read_b64_tr_b16 v[174:175], v205 offset:25088\n"
    "v_cvt_pk_bf16_f32 v42, v24, v25\n"
    "s_waitcnt lgkmcnt(3)\n"
    "v_mfma_f32_16x16x32_bf16 v[64:67], v[160:163], a[96:99], v[64:67]\n"
    "buffer_store_dwordx4 v[36:39], v246, s[36:39], s90 offen offset:2048 sc1\n"
    "v_cvt_pk_bf16_f32 v43, v26, v27\n"
    "v_mfma_f32_16x16x32_bf16 v[64:67], v[164:167], a[112:115], v[64:67]\n"
    "v_mul_f32 v12, v12, v44\n"
    "v_mul_f32 v13, v13, v45\n"
    "v_mfma_f32_16x16x32_bf16 v[68:71], v[160:163], a[100:103], v[68:71]\n"
    "buffer_load_dwordx4 v201, s[32:35], s86 offen offset:3072 lds\n"
    "ds_read_b64_tr_b16 v[176:177], v204 offset:18432\n"
    "v_mfma_f32_16x16x32_bf16 v[68:71], v[164:167], a[116:119], v[68:71]\n"
    "ds_read_b64_tr_b16 v[178:179], v204 offset:18944\n"
    "v_mul_f32 v14, v14, v46\n"
    "v_mfma_f32_16x16x32_bf16 v[72:75], v[160:163], a[104:107], v[72:75]\n"
    "ds_read_b64_tr_b16 v[180:181], v204 offset:26624\n"
    "s_add_u32 s98, s78, 128\n"
    "v_mul_f32 v15, v15, v47\n"
    "v_mfma_f32_16x16x32_bf16 v[72:75], v[164:167], a[120:123], v[72:75]\n"
    "ds_read_b64_tr_b16 v[182:183], v204 offset:27136\n"
    "s_add_u32 m0, s63, 131072\n"
    "v_mul_f32 v28, v28, v60\n"
    "v_mfma_f32_16x16x32_bf16 v[76:79], v[160:163], a[108:111], v[76:79]\n"
    "buffer_load_dwordx4 v196, s[24:27], s98 offen offset:0 lds\n"
    "v_mul_f32 v29, v29, v61\n"
    "v_mfma_f32_16x16x32_bf16 v[76:79], v[164:167], a[124:127], v[76:79]\n"
    "v_mul_f32 v30, v30, v62\n"
    "v_mul_f32 v31, v31, v63\n"
    "s_waitcnt lgkmcnt(2)\n"
    "v_mfma_f32_16x16x32_bf16 v[80:83], v[168:171], a[96:99], v[80:83]\n"
    "v_cvt_pk_bf16_f32 v44, v12, v13\n"
    "v_cvt_pk_bf16_f32 v45, v14, v15\n"
    "v_mfma_f32_16x16x32_bf16 v[80:83], v[172:175], a[112:115], v[80:83]\n"
    "v_cvt_pk_bf16_f32 v46, v28, v29\n"
    "v_cvt_pk_bf16_f32 v47, v30, v31\n"
    "v_mfma_f32_16x16x32_bf16 v[84:87], v[168:171], a[100:103], v[84:87]\n"
    "ds_read_b64_tr_b16 v[184:185], v205 offset:18432\n"
    "v_mfma_f32_16x16x32_bf16 v[84:87], v[172:175], a[116:119], v[84:87]\n"
    "buffer_load_dwordx4 v197, s[24:27], s98 offen offset:1024 lds\n"
    "ds_read_b64_tr_b16 v[186:187], v205 offset:18944\n"
    "v_mfma_f32_16x16x32_bf16 v[88:91], v[168:171], a[104:107], v[88:91]\n"
    "ds_read_b64_tr_b16 v[188:189], v205 offset:26624\n"
    "v_mfma_f32_16x16x32_bf16 v[88:91], v[172:175], a[120:123], v[88:91]\n"
    "ds_read_b64_tr_b16 v[190:191], v205 offset:27136\n"
    "v_mfma_f32_16x16x32_bf16 v[92:95], v[168:171], a[108:111], v[92:95]\n"
    "ds_read_b64_tr_b16 a[64:65], v218 offset:0\n"
    "v_mfma_f32_16x16x32_bf16 v[92:95], v[172:175], a[124:127], v[92:95]\n"
    "buffer_load_dwordx4 v196, s[28:31], s98 offen offset:2048 lds\n"
    "ds_read_b64_tr_b16 a[66:67], v218 offset:2048\n"
    "v_mfma_f32_16x16x32_bf16 v[96:99], v[176:179], a[96:99], v[96:99]\n"
    "ds_read_b64_tr_b16 a[68:69], v219 offset:0\n"
    "s_waitcnt lgkmcnt(4)\n"
    "v_mfma_f32_16x16x32_bf16 v[96:99], v[180:183], a[112:115], v[96:99]\n"
    "ds_read_b64_tr_b16 a[70:71], v219 offset:2048\n"
    "v_mfma_f32_16x16x32_bf16 v[100:103], v[176:179], a[100:103], v[100:103]\n"
    "ds_read_b64_tr_b16 a[72:73], v220 offset:0\n"
    "v_mfma_f32_16x16x32_bf16 v[100:103], v[180:183], a[116:119], v[100:103]\n"
    "ds_read_b64_tr_b16 a[74:75], v220 offset:2048\n"
    "buffer_store_dwordx4 v[40:43], v246, s[36:39], s91 offen offset:0 sc1\n"
    "v_mfma_f32_16x16x32_bf16 v[104:107], v[176:179], a[104:107], v[104:107]\n"
    "s_waitcnt vmcnt(12)\n"
    "s_barrier\n"
    "ds_read_b64_tr_b16 v[128:129], v202 offset:0\n"
    "v_mfma_f32_16x16x32_bf16 v[104:107], v[180:183], a[120:123], v[104:107]\n"
    "buffer_load_dwordx4 v197, s[28:31], s98 offen offset:3072 lds\n"
    "ds_read_b64_tr_b16 v[130:131], v202 offset:512\n"
    "v_mfma_f32_16x16x32_bf16 v[108:111], v[176:179], a[108:111], v[108:111]\n"
    "ds_read_b64_tr_b16 v[132:133], v202 offset:8192\n"
    "v_mfma_f32_16x16x32_bf16 v[108:111], v[180:183], a[124:127], v[108:111]\n"
    "ds_read_b64_tr_b16 v[134:135], v202 offset:8704\n"
    "s_add_u32 s99, s80, 65536\n"
    "v_mfma_f32_16x16x32_bf16 v[112:115], v[184:187], a[96:99], v[112:115]\n"
    "ds_read_b64_tr_b16 v[136:137], v203 offset:0\n"
    "s_waitcnt lgkmcnt(4)\n"
    "v_mfma_f32_16x16x32_bf16 v[112:115], v[188:191], a[112:115], v[112:115]\n"
    "ds_read_b64_tr_b16 v[138:139], v203 offset:512\n"
    "s_add_u32 m0, s63, 16384\n"
    "v_mfma_f32_16x16x32_bf16 v[116:119], v[184:187], a[100:103], v[116:119]\n"
    "buffer_load_dwordx4 v198, s[32:35], s99 offen offset:0 lds\n"
    "ds_read_b64_tr_b16 v[140:141], v203 offset:8192\n"
    "v_mfma_f32_16x16x32_bf16 v[116:119], v[188:191], a[116:119], v[116:119]\n"
    "ds_read_b64_tr_b16 v[142:143], v203 offset:8704\n"
    "v_mfma_f32_16x16x32_bf16 v[120:123], v[184:187], a[104:107], v[120:123]\n"
    "ds_read_b64_tr_b16 a[76:77], v221 offset:0\n"
    "v_mfma_f32_16x16x32_bf16 v[120:123], v[188:191], a[120:123], v[120:123]\n"
    "buffer_load_dwordx4 v199, s[32:35], s99 offen offset:1024 lds\n"
    "ds_read_b64_tr_b16 a[78:79], v221 offset:2048\n"
    "v_mfma_f32_16x16x32_bf16 v[124:127], v[184:187], a[108:111], v[124:127]\n"
    "ds_read_b64_tr_b16 a[80:81], v218 offset:4096\n"
    "v_mfma_f32_16x16x32_bf16 v[124:127], v[188:191], a[124:127], v[124:127]\n"
    "ds_read_b64_tr_b16 a[82:83], v218 offset:6144\n"
    "s_waitcnt lgkmcnt(4)\n"
    "v_mfma_f32_16x16x32_bf16 v[64:67], v[128:131], a[128:131], v[64:67]\n"
    "ds_read_b64_tr_b16 v[144:145], v202 offset:2048\n"
    "v_mfma_f32_16x16x32_bf16 v[64:67], v[132:135], a[144:147], v[64:67]\n"
    "ds_read_b64_tr_b16 v[146:147], v202 offset:2560\n"
    "v_mfma_f32_16x16x32_bf16 v[68:71], v[128:131], a[132:135], v[68:71]\n"
    "buffer_load_dwordx4 v200, s[32:35], s99 offen offset:2048 lds\n"
    "ds_read_b64_tr_b16 v[148:149], v202 offset:10240\n"
    "v_mfma_f32_16x16x32_bf16 v[68:71], v[132:135], a[148:151], v[68:71]\n"
    "ds_read_b64_tr_b16 v[150:151], v202 offset:10752\n"
    "v_mfma_f32_16x16x32_bf16 v[72:75], v[128:131], a[136:139], v[72:75]\n"
    "ds_read_b64_tr_b16 a[84:85], v219 offset:4096\n"
    "v_mfma_f32_16x16x32_bf16 v[72:75], v[132:135], a[152:155], v[72:75]\n"
    "ds_read_b64_tr_b16 a[86:87], v219 offset:6144\n"
    "v_mfma_f32_16x16x32_bf16 v[76:79], v[128:131], a[140:143], v[76:79]\n"
    "buffer_load_dwordx4 v201, s[32:35], s99 offen offset:3072 lds\n"
    "ds_read_b64_tr_b16 a[88:89], v220 offset:4096\n"
    "v_mfma_f32_16x16x32_bf16 v[76:79], v[132:135], a[156:159], v[76:79]\n"
    "ds_read_b64_tr_b16 a[90:91], v220 offset:6144\n"
    "buffer_store_dwordx4 v[44:47], v246, s[36:39], s91 offen offset:2048 sc1\n"
    "v_mfma_f32_16x16x32_bf16 v[80:83], v[136:139], a[128:131], v[80:83]\n"
    "ds_read_b64_tr_b16 v[152:153], v203 offset:2048\n"
    "s_add_u32 s91, s91, 8192\n"
    "v_mfma_f32_16x16x32_bf16 v[80:83], v[140:143], a[144:147], v[80:83]\n"
    "ds_read_b64_tr_b16 v[154:155], v203 offset:2560\n"
    "s_add_u32 s90, s90, 8192\n"
    "v_mfma_f32_16x16x32_bf16 v[84:87], v[136:139], a[132:135], v[84:87]\n"
    "ds_read_b64_tr_b16 v[156:157], v203 offset:10240\n"
    "v_mfma_f32_16x16x32_bf16 v[84:87], v[140:143], a[148:151], v[84:87]\n"
    "ds_read_b64_tr_b16 v[158:159], v203 offset:10752\n"
    "v_mfma_f32_16x16x32_bf16 v[88:91], v[136:139], a[136:139], v[88:91]\n"
    "ds_read_b64_tr_b16 a[92:93], v221 offset:4096\n"
    "v_mfma_f32_16x16x32_bf16 v[88:91], v[140:143], a[152:155], v[88:91]\n"
    "ds_read_b64_tr_b16 a[94:95], v221 offset:6144\n"
    "v_mfma_f32_16x16x32_bf16 v[92:95], v[136:139], a[140:143], v[92:95]\n"
    "ds_read_b64_tr_b16 v[0:1], v214 offset:49152\n"
    "v_mfma_f32_16x16x32_bf16 v[92:95], v[140:143], a[156:159], v[92:95]\n"
    "ds_read_b64_tr_b16 v[2:3], v214 offset:51200\n"
    "s_waitcnt lgkmcnt(4)\n"
    "v_mfma_f32_16x16x32_bf16 v[96:99], v[144:147], a[128:131], v[96:99]\n"
    "ds_read_b64_tr_b16 v[4:5], v215 offset:49152\n"
    "v_mfma_f32_16x16x32_bf16 v[96:99], v[148:151], a[144:147], v[96:99]\n"
    "ds_read_b64_tr_b16 v[6:7], v215 offset:51200\n"
    "v_mfma_f32_16x16x32_bf16 v[100:103], v[144:147], a[132:135], v[100:103]\n"
    "ds_read_b64_tr_b16 v[8:9], v216 offset:49152\n"
    "v_mfma_f32_16x16x32_bf16 v[100:103], v[148:151], a[148:151], v[100:103]\n"
    "ds_read_b64_tr_b16 v[10:11], v216 offset:51200\n"
    "v_mfma_f32_16x16x32_bf16 v[104:107], v[144:147], a[136:139], v[104:107]\n"
    "s_waitcnt vmcnt(12)\n"
    "s_barrier\n"
    "ds_read_b64_tr_b16 v[160:161], v202 offset:32768\n"
    "v_mfma_f32_16x16x32_bf16 v[104:107], v[148:151], a[152:155], v[104:107]\n"
    "ds_read_b64_tr_b16 v[162:163], v202 offset:33280\n"
    "s_add_u32 s98, s79, 128\n"
    "s_add_u32 m0, s63, 0\n"
    "v_mfma_f32_16x16x32_bf16 v[108:111], v[144:147], a[140:143], v[108:111]\n"
    "buffer_load_dwordx4 v196, s[24:27], s98 offen offset:0 lds\n"
    "ds_read_b64_tr_b16 v[164:165], v202 offset:40960\n"
    "v_mfma_f32_16x16x32_bf16 v[108:111], v[148:151], a[156:159], v[108:111]\n"
    "ds_read_b64_tr_b16 v[166:167], v202 offset:41472\n"
    "v_mfma_f32_16x16x32_bf16 v[112:115], v[152:155], a[128:131], v[112:115]\n"
    "buffer_load_dwordx4 v197, s[24:27], s98 offen offset:1024 lds\n"
    "ds_read_b64_tr_b16 v[168:169], v203 offset:32768\n"
    "v_mfma_f32_16x16x32_bf16 v[112:115], v[156:159], a[144:147], v[112:115]\n"
    "ds_read_b64_tr_b16 v[170:171], v203 offset:33280\n"
    "v_mfma_f32_16x16x32_bf16 v[116:119], v[152:155], a[132:135], v[116:119]\n"
    "buffer_load_dwordx4 v196, s[28:31], s98 offen offset:2048 lds\n"
    "ds_read_b64_tr_b16 v[172:173], v203 offset:40960\n"
    "v_mfma_f32_16x16x32_bf16 v[116:119], v[156:159], a[148:151], v[116:119]\n"
    "ds_read_b64_tr_b16 v[174:175], v203 offset:41472\n"
    "v_mfma_f32_16x16x32_bf16 v[120:123], v[152:155], a[136:139], v[120:123]\n"
    "buffer_load_dwordx4 v197, s[28:31], s98 offen offset:3072 lds\n"
    "ds_read_b64_tr_b16 v[12:13], v217 offset:49152\n"
    "v_mfma_f32_16x16x32_bf16 v[120:123], v[156:159], a[152:155], v[120:123]\n"
    "ds_read_b64_tr_b16 v[14:15], v217 offset:51200\n"
    "s_add_u32 s99, s81, 65536\n"
    "s_add_u32 m0, s63, 81920\n"
    "v_mfma_f32_16x16x32_bf16 v[124:127], v[152:155], a[140:143], v[124:127]\n"
    "buffer_load_dwordx4 v198, s[32:35], s99 offen offset:0 lds\n"
    "ds_read_b64_tr_b16 v[16:17], v214 offset:53248\n"
    "v_mfma_f32_16x16x32_bf16 v[124:127], v[156:159], a[156:159], v[124:127]\n"
    "ds_read_b64_tr_b16 v[18:19], v214 offset:55296\n"
    "s_waitcnt lgkmcnt(4)\n"
    "v_mfma_f32_16x16x32_bf16 v[64:67], v[160:163], a[160:163], v[64:67]\n"
    "ds_read_b64_tr_b16 v[176:177], v202 offset:34816\n"
    "v_mfma_f32_16x16x32_bf16 v[64:67], v[164:167], a[176:179], v[64:67]\n"
    "buffer_load_dwordx4 v199, s[32:35], s99 offen offset:1024 lds\n"
    "ds_read_b64_tr_b16 v[178:179], v202 offset:35328\n"
    "v_mfma_f32_16x16x32_bf16 v[68:71], v[160:163], a[164:167], v[68:71]\n"
    "ds_read_b64_tr_b16 v[180:181], v202 offset:43008\n"
    "v_mfma_f32_16x16x32_bf16 v[68:71], v[164:167], a[180:183], v[68:71]\n"
    "ds_read_b64_tr_b16 v[182:183], v202 offset:43520\n"
    "v_mfma_f32_16x16x32_bf16 v[72:75], v[160:163], a[168:171], v[72:75]\n"
    "ds_read_b64_tr_b16 v[20:21], v215 offset:53248\n"
    "v_mfma_f32_16x16x32_bf16 v[72:75], v[164:167], a[184:187], v[72:75]\n"
    "buffer_load_dwordx4 v200, s[32:35], s99 offen offset:2048 lds\n"
    "ds_read_b64_tr_b16 v[22:23], v215 offset:55296\n"
    "v_mfma_f32_16x16x32_bf16 v[76:79], v[160:163], a[172:175], v[76:79]\n"
    "ds_read_b64_tr_b16 v[24:25], v216 offset:53248\n"
    "v_mfma_f32_16x16x32_bf16 v[76:79], v[164:167], a[188:191], v[76:79]\n"
    "ds_read_b64_tr_b16 v[26:27], v216 offset:55296\n"
    "v_mfma_f32_16x16x32_bf16 v[80:83], v[168:171], a[160:163], v[80:83]\n"
    "ds_read_b64_tr_b16 v[184:185], v203 offset:34816\n"
    "v_mfma_f32_16x16x32_bf16 v[80:83], v[172:175], a[176:179], v[80:83]\n"
    "buffer_load_dwordx4 v201, s[32:35], s99 offen offset:3072 lds\n"
    "ds_read_b64_tr_b16 v[186:187], v203 offset:35328\n"
    "v_mfma_f32_16x16x32_bf16 v[84:87], v[168:171], a[164:167], v[84:87]\n"
    "ds_read_b64_tr_b16 v[188:189], v203 offset:43008\n"
    "v_mfma_f32_16x16x32_bf16 v[84:87], v[172:175], a[180:183], v[84:87]\n"
    "ds_read_b64_tr_b16 v[190:191], v203 offset:43520\n"
    "v_mfma_f32_16x16x32_bf16 v[88:91], v[168:171], a[168:171], v[88:91]\n"
    "ds_read_b64_tr_b16 v[28:29], v217 offset:53248\n"
    "v_mfma_f32_16x16x32_bf16 v[88:91], v[172:175], a[184:187], v[88:91]\n"
    "ds_read_b64_tr_b16 v[30:31], v217 offset:55296\n"
    "v_mfma_f32_16x16x32_bf16 v[92:95], v[168:171], a[172:175], v[92:95]\n"
    "s_waitcnt vmcnt(8)\n"
    "s_barrier\n"
    "s_cmp_lg_u32 s88, 0\n"
    "s_cbranch_scc1 .Lg_noflag_2\n"
    "buffer_store_dwordx4 v[228:231], v227, s[36:39], s89 offen sc1\n"
    ".Lg_noflag_2:\n"
    "v_mfma_f32_16x16x32_bf16 v[92:95], v[172:175], a[188:191], v[92:95]\n"
    "ds_read_b64_tr_b16 a[96:97], v222 offset:16384\n"
    "s_add_u32 s98, s82, 65536\n"
    "s_waitcnt lgkmcnt(3)\n"
    "v_mfma_f32_16x16x32_bf16 v[96:99], v[176:179], a[160:163], v[96:99]\n"
    "ds_read_b64_tr_b16 v[128:129], v206 offset:0\n"
    "s_add_u32 m0, s63, 65536\n"
    "v_mfma_f32_16x16x32_bf16 v[96:99], v[180:183], a[176:179], v[96:99]\n"
    "buffer_load_dwordx4 v198, s[32:35], s98 offen offset:0 lds\n"
    "ds_read_b64_tr_b16 v[130:131], v206 offset:512\n"
    "v_mfma_f32_16x16x32_bf16 v[100:103], v[176:179], a[164:167], v[100:103]\n"
    "ds_read_b64_tr_b16 v[132:133], v206 offset:8192\n"
    "v_mfma_f32_16x16x32_bf16 v[100:103], v[180:183], a[180:183], v[100:103]\n"
    "buffer_load_dwordx4 v199, s[32:35], s98 offen offset:1024 lds\n"
    "ds_read_b64_tr_b16 v[134:135], v206 offset:8704\n"
    "v_mfma_f32_16x16x32_bf16 v[104:107], v[176:179], a[168:171], v[104:107]\n"
    "s_barrier\n"
    "ds_read_b64_tr_b16 a[98:99], v222 offset:18432\n"
    "v_mfma_f32_16x16x32_bf16 v[104:107], v[180:183], a[184:187], v[104:107]\n"
    "buffer_load_dwordx4 v200, s[32:35], s98 offen offset:2048 lds\n"
    "ds_read_b64_tr_b16 a[100:101], v223 offset:16384\n"
    "v_mfma_f32_16x16x32_bf16 v[108:111], v[176:179], a[172:175], v[108:111]\n"
    "ds_read_b64_tr_b16 a[102:103], v223 offset:18432\n"
    "v_mfma_f32_16x16x32_bf16 v[108:111], v[180:183], a[188:191], v[108:111]\n"
    "ds_read_b64_tr_b16 a[104:105], v224 offset:16384\n"
    "v_mfma_f32_16x16x32_bf16 v[112:115], v[184:187], a[160:163], v[112:115]\n"
    "ds_read_b64_tr_b16 v[136:137], v207 offset:0\n"
    "v_mfma_f32_16x16x32_bf16 v[112:115], v[188:191], a[176:179], v[112:115]\n"
    "buffer_load_dwordx4 v201, s[32:35], s98 offen offset:3072 lds\n"
    "ds_read_b64_tr_b16 v[138:139], v207 offset:512\n"
    "v_mfma_f32_16x16x32_bf16 v[116:119], v[184:187], a[164:167], v[116:119]\n"
    "ds_read_b64_tr_b16 v[140:141], v207 offset:8192\n"
    "v_mfma_f32_16x16x32_bf16 v[116:119], v[188:191], a[180:183], v[116:119]\n"
    "ds_read_b64_tr_b16 v[142:143], v207 offset:8704\n"
    "s_add_u32 s99, s83, 65536\n"
    "v_mfma_f32_16x16x32_bf16 v[120:123], v[184:187], a[168:171], v[120:123]\n"
    "ds_read_b64_tr_b16 a[106:107], v224 offset:18432\n"
    "s_add_u32 m0, s63, 32768\n"
    "v_mfma_f32_16x16x32_bf16 v[120:123], v[188:191], a[184:187], v[120:123]\n"
    "buffer_load_dwordx4 v198, s[32:35], s99 offen offset:0 lds\n"
    "ds_read_b64_tr_b16 a[108:109], v225 offset:16384\n"
    "v_mfma_f32_16x16x32_bf16 v[124:127], v[184:187], a[172:175], v[124:127]\n"
    "ds_read_b64_tr_b16 a[110:111], v225 offset:18432\n"
    "v_mfma_f32_16x16x32_bf16 v[124:127], v[188:191], a[188:191], v[124:127]\n"
    "ds_read_b64_tr_b16 a[112:113], v222 offset:20480\n"
    "s_waitcnt lgkmcnt(4)\n"
    "v_mfma_f32_16x16x32_bf16 v[64:67], v[128:131], a[0:3], v[64:67]\n"
    "ds_read_b64_tr_b16 v[144:145], v206 offset:2048\n"
    "v_mfma_f32_16x16x32_bf16 v[64:67], v[132:135], a[16:19], v[64:67]\n"
    "buffer_load_dwordx4 v199, s[32:35], s99 offen offset:1024 lds\n"
    "ds_read_b64_tr_b16 v[146:147], v206 offset:2560\n"
    "v_mfma_f32_16x16x32_bf16 v[68:71], v[128:131], a[4:7], v[68:71]\n"
    "ds_read_b64_tr_b16 v[148:149], v206 offset:10240\n"
    "v_mfma_f32_16x16x32_bf16 v[68:71], v[132:135], a[20:23], v[68:71]\n"
    "ds_read_b64_tr_b16 v[150:151], v206 offset:10752\n"
    "v_mfma_f32_16x16x32_bf16 v[72:75], v[128:131], a[8:11], v[72:75]\n"
    "ds_read_b64_tr_b16 a[114:115], v222 offset:22528\n"
    "v_mfma_f32_16x16x32_bf16 v[72:75], v[132:135], a[24:27], v[72:75]\n"
    "buffer_load_dwordx4 v200, s[32:35], s99 offen offset:2048 lds\n"
    "ds_read_b64_tr_b16 a[116:117], v223 offset:20480\n"
    "v_mfma_f32_16x16x32_bf16 v[76:79], v[128:131], a[12:15], v[76:79]\n"
    "ds_read_b64_tr_b16 a[118:119], v223 offset:22528\n"
    "v_mfma_f32_16x16x32_bf16 v[76:79], v[132:135], a[28:31], v[76:79]\n"
    "ds_read_b64_tr_b16 a[120:121], v224 offset:20480\n"
    "v_mfma_f32_16x16x32_bf16 v[80:83], v[136:139], a[0:3], v[80:83]\n"
    "s_waitcnt vmcnt(11)\n"
    "s_barrier\n"
    "ds_read_b64_tr_b16 v[152:153], v207 offset:2048\n"
    "v_mfma_f32_16x16x32_bf16 v[80:83], v[140:143], a[16:19], v[80:83]\n"
    "buffer_load_dwordx4 v201, s[32:35], s99 offen offset:3072 lds\n"
    "ds_read_b64_tr_b16 v[154:155], v207 offset:2560\n"
    "v_mfma_f32_16x16x32_bf16 v[84:87], v[136:139], a[4:7], v[84:87]\n"
    "ds_read_b64_tr_b16 v[156:157], v207 offset:10240\n"
    "v_mfma_f32_16x16x32_bf16 v[84:87], v[140:143], a[20:23], v[84:87]\n"
    "ds_read_b64_tr_b16 v[158:159], v207 offset:10752\n"
    "s_add_u32 s98, s84, 65536\n"
    "v_mfma_f32_16x16x32_bf16 v[88:91], v[136:139], a[8:11], v[88:91]\n"
    "ds_read_b64_tr_b16 a[122:123], v224 offset:22528\n"
    "s_add_u32 m0, s63, 49152\n"
    "v_mfma_f32_16x16x32_bf16 v[88:91], v[140:143], a[24:27], v[88:91]\n"
    "buffer_load_dwordx4 v198, s[32:35], s98 offen offset:0 lds\n"
    "ds_read_b64_tr_b16 a[124:125], v225 offset:20480\n"
    "v_mfma_f32_16x16x32_bf16 v[92:95], v[136:139], a[12:15], v[92:95]\n"
    "ds_read_b64_tr_b16 a[126:127], v225 offset:22528\n"
    "v_mfma_f32_16x16x32_bf16 v[92:95], v[140:143], a[28:31], v[92:95]\n"
    "ds_read_b64_tr_b16 v[32:33], v218 offset:32768\n"
    "s_waitcnt lgkmcnt(4)\n"
    "v_mfma_f32_16x16x32_bf16 v[96:99], v[144:147], a[0:3], v[96:99]\n"
    "ds_read_b64_tr_b16 v[160:161], v202 offset:0\n"
    "v_mfma_f32_16x16x32_bf16 v[96:99], v[148:151], a[16:19], v[96:99]\n"
    "buffer_load_dwordx4 v199, s[32:35], s98 offen offset:1024 lds\n"
    "ds_read_b64_tr_b16 v[162:163], v202 offset:512\n"
    "v_mfma_f32_16x16x32_bf16 v[100:103], v[144:147], a[4:7], v[100:103]\n"
    "ds_read_b64_tr_b16 v[164:165], v203 offset:0\n"
    "v_mfma_f32_16x16x32_bf16 v[100:103], v[148:151], a[20:23], v[100:103]\n"
    "ds_read_b64_tr_b16 v[166:167], v203 offset:512\n"
    "v_mfma_f32_16x16x32_bf16 v[104:107], v[144:147], a[8:11], v[104:107]\n"
    "ds_read_b64_tr_b16 v[168:169], v202 offset:2048\n"
    "v_mfma_f32_16x16x32_bf16 v[104:107], v[148:151], a[24:27], v[104:107]\n"
    "buffer_load_dwordx4 v200, s[32:35], s98 offen offset:2048 lds\n"
    "ds_read_b64_tr_b16 v[170:171], v202 offset:2560\n"
    "v_mfma_f32_16x16x32_bf16 v[108:111], v[144:147], a[12:15], v[108:111]\n"
    "ds_read_b64_tr_b16 v[172:173], v203 offset:2048\n"
    "v_mfma_f32_16x16x32_bf16 v[108:111], v[148:151], a[28:31], v[108:111]\n"
    "ds_read_b64_tr_b16 v[174:175], v203 offset:2560\n"
    "v_mfma_f32_16x16x32_bf16 v[112:115], v[152:155], a[0:3], v[112:115]\n"
    "ds_read_b64_tr_b16 v[176:177], v202 offset:8192\n"
    "v_mfma_f32_16x16x32_bf16 v[112:115], v[156:159], a[16:19], v[112:115]\n"
    "buffer_load_dwordx4 v201, s[32:35], s98 offen offset:3072 lds\n"
    "ds_read_b64_tr_b16 v[178:179], v202 offset:8704\n"
    "v_mfma_f32_16x16x32_bf16 v[116:119], v[152:155], a[4:7], v[116:119]\n"
    "ds_read_b64_tr_b16 v[180:181], v203 offset:8192\n"
    "v_mfma_f32_16x16x32_bf16 v[116:119], v[156:159], a[20:23], v[116:119]\n"
    "ds_read_b64_tr_b16 v[182:183], v203 offset:8704\n"
    "v_mfma_f32_16x16x32_bf16 v[120:123], v[152:155], a[8:11], v[120:123]\n"
    "s_waitcnt vmcnt(12)\n"
    "s_barrier\n"
    "ds_read_b64_tr_b16 v[184:185], v202 offset:10240\n"
    "v_mfma_f32_16x16x32_bf16 v[120:123], v[156:159], a[24:27], v[120:123]\n"
    "ds_read_b64_tr_b16 v[186:187], v202 offset:10752\n"
    "s_add_u32 s99, s85, 65536\n"
    "s_add_u32 m0, s63, 131072\n"
    "v_mfma_f32_16x16x32_bf16 v[124:127], v[152:155], a[12:15], v[124:127]\n"
    "buffer_load_dwordx4 v198, s[32:35], s99 offen offset:0 lds\n"
    "ds_read_b64_tr_b16 v[188:189], v203 offset:10240\n"
    "v_mfma_f32_16x16x32_bf16 v[124:127], v[156:159], a[28:31], v[124:127]\n"
    "ds_read_b64_tr_b16 v[190:191], v203 offset:10752\n"
    "s_waitcnt lgkmcnt(4)\n"
    "v_mfma_f32_16x16x32_bf16 v[64:67], v[160:163], a[32:35], v[64:67]\n"
    "ds_read_b64_tr_b16 v[34:35], v218 offset:34816\n"
    "v_mfma_f32_16x16x32_bf16 v[80:83], v[164:167], a[32:35], v[80:83]\n"
    "ds_read_b64_tr_b16 v[36:37], v219 offset:32768\n"
    "v_mfma_f32_16x16x32_bf16 v[96:99], v[168:171], a[32:35], v[96:99]\n"
    "buffer_load_dwordx4 v199, s[32:35], s99 offen offset:1024 lds\n"
    "ds_read_b64_tr_b16 v[38:39], v219 offset:34816\n"
    "v_mfma_f32_16x16x32_bf16 v[112:115], v[172:175], a[32:35], v[112:115]\n"
    "ds_read_b64_tr_b16 v[40:41], v220 offset:32768\n"
    "v_mfma_f32_16x16x32_bf16 v[64:67], v[176:179], a[48:51], v[64:67]\n"
    "ds_read_b64_tr_b16 v[42:43], v220 offset:34816\n"
    "v_mfma_f32_16x16x32_bf16 v[80:83], v[180:183], a[48:51], v[80:83]\n"
    "ds_read_b64_tr_b16 v[44:45], v221 offset:32768\n"
    "s_waitcnt lgkmcnt(4)\n"
    "v_mfma_f32_16x16x32_bf16 v[96:99], v[184:187], a[48:51], v[96:99]\n"
    "buffer_load_dwordx4 v200, s[32:35], s99 offen offset:2048 lds\n"
    "ds_read_b64_tr_b16 v[46:47], v221 offset:34816\n"
    "v_mfma_f32_16x16x32_bf16 v[112:115], v[188:191], a[48:51], v[112:115]\n"
    "ds_read_b64_tr_b16 v[48:49], v218 offset:36864\n"
    "s_nop 0\n"
    "v_mfma_f32_16x16x32_bf16 v[68:71], v[160:163], a[36:39], v[68:71]\n"
    "ds_read_b64_tr_b16 v[50:51], v218 offset:38912\n"
    "s_nop 0\n"
    "v_mfma_f32_16x16x32_bf16 v[84:87], v[164:167], a[36:39], v[84:87]\n"
    "ds_read_b64_tr_b16 v[52:53], v219 offset:36864\n"
    "s_nop 0\n"
    "v_mul_f32 v64, v64, v96\n"
    "v_mfma_f32_16x16x32_bf16 v[100:103], v[168:171], a[36:39], v[100:103]\n"
    "v_mul_f32 v65, v65, v97\n"
    "v_mul_f32 v66, v66, v98\n"
    "v_mfma_f32_16x16x32_bf16 v[116:119], v[172:175], a[36:39], v[116:119]\n"
    "buffer_load_dwordx4 v201, s[32:35], s99 offen offset:3072 lds\n"
    "v_mul_f32 v67, v67, v99\n"
    "v_mfma_f32_16x16x32_bf16 v[68:71], v[176:179], a[52:55], v[68:71]\n"
    "v_mul_f32 v80, v80, v112\n"
    "v_mul_f32 v81, v81, v113\n"
    "v_mfma_f32_16x16x32_bf16 v[84:87], v[180:183], a[52:55], v[84:87]\n"
    "v_mul_f32 v82, v82, v114\n"
    "v_mul_f32 v83, v83, v115\n"
    "v_mfma_f32_16x16x32_bf16 v[100:103], v[184:187], a[52:55], v[100:103]\n"
    "v_cvt_pk_bf16_f32 v96, v64, v65\n"
    "v_cvt_pk_bf16_f32 v97, v66, v67\n"
    "v_mfma_f32_16x16x32_bf16 v[116:119], v[188:191], a[52:55], v[116:119]\n"
    "v_cvt_pk_bf16_f32 v98, v80, v81\n"
    "v_cvt_pk_bf16_f32 v99, v82, v83\n"
    "v_mfma_f32_16x16x32_bf16 v[72:75], v[160:163], a[40:43], v[72:75]\n"
    "s_waitcnt lgkmcnt(0)\n"
    "s_load_dwordx2 s[64:65], s[96:97], 0x0 glc\n"
    "s_load_dwordx2 s[66:67], s[96:97], 0x100 glc\n"
    "s_load_dwordx2 s[68:69], s[96:97], 0x200 glc\n"
    "s_load_dwordx2 s[70:71], s[96:97], 0x300 glc\n"
    "s_load_dwordx2 s[72:73], s[96:97], 0x400 glc\n"
    "s_load_dwordx2 s[74:75], s[96:97], 0x500 glc\n"
    "s_load_dwordx2 s[76:77], s[96:97], 0x600 glc\n"
    "v_mfma_f32_16x16x32_bf16 v[88:91], v[164:167], a[40:43], v[88:91]\n"
    "buffer_store_dwordx4 v[96:99], v246, s[36:39], s90 offen offset:0 sc1\n"
    "v_mul_f32 v68, v68, v100\n"
    "v_mfma_f32_16x16x32_bf16 v[104:107], v[168:171], a[40:43], v[104:107]\n"
    "v_mul_f32 v69, v69, v101\n"
    "v_mul_f32 v70, v70, v102\n"
    "v_mfma_f32_16x16x32_bf16 v[120:123], v[172:175], a[40:43], v[120:123]\n"
    "v_mul_f32 v71, v71, v103\n"
    "v_mul_f32 v84, v84, v116\n"
    "v_mfma_f32_16x16x32_bf16 v[72:75], v[176:179], a[56:59], v[72:75]\n"
    "v_mul_f32 v85, v85, v117\n"
    "v_mul_f32 v86, v86, v118\n"
    "v_mfma_f32_16x16x32_bf16 v[88:91], v[180:183], a[56:59], v[88:91]\n"
    "v_mul_f32 v87, v87, v119\n"
    "v_cvt_pk_bf16_f32 v100, v68, v69\n"
    "v_mfma_f32_16x16x32_bf16 v[104:107], v[184:187], a[56:59], v[104:107]\n"
    "v_cvt_pk_bf16_f32 v101, v70, v71\n"
    "v_cvt_pk_bf16_f32 v102, v84, v85\n"
    "v_mfma_f32_16x16x32_bf16 v[120:123], v[188:191], a[56:59], v[120:123]\n"
    "v_cvt_pk_bf16_f32 v103, v86, v87\n"
    "buffer_store_dwordx4 v[100:103], v246, s[36:39], s90 offen offset:2048 sc1\n"
    "v_mfma_f32_16x16x32_bf16 v[76:79], v[160:163], a[44:47], v[76:79]\n"
    "s_nop 2\n"
    "v_mfma_f32_16x16x32_bf16 v[92:95], v[164:167], a[44:47], v[92:95]\n"
    "s_nop 0\n"
    "v_mul_f32 v72, v72, v104\n"
    "v_mul_f32 v73, v73, v105\n"
    "v_mfma_f32_16x16x32_bf16 v[108:111], v[168:171], a[44:47], v[108:111]\n"
    "v_mul_f32 v74, v74, v106\n"
    "v_mul_f32 v75, v75, v107\n"
    "v_mfma_f32_16x16x32_bf16 v[124:127], v[172:175], a[44:47], v[124:127]\n"
    "v_mul_f32 v88, v88, v120\n"
    "v_mul_f32 v89, v89, v121\n"
    "v_mfma_f32_16x16x32_bf16 v[76:79], v[176:179], a[60:63], v[76:79]\n"
    "v_mul_f32 v90, v90, v122\n"
    "v_mul_f32 v91, v91, v123\n"
    "v_mfma_f32_16x16x32_bf16 v[92:95], v[180:183], a[60:63], v[92:95]\n"
    "v_cvt_pk_bf16_f32 v104, v72, v73\n"
    "v_cvt_pk_bf16_f32 v105, v74, v75\n"
    "v_mfma_f32_16x16x32_bf16 v[108:111], v[184:187], a[60:63], v[108:111]\n"
    "v_cvt_pk_bf16_f32 v106, v88, v89\n"
    "v_cvt_pk_bf16_f32 v107, v90, v91\n"
    "v_mfma_f32_16x16x32_bf16 v[124:127], v[188:191], a[60:63], v[124:127]\n"
    "buffer_store_dwordx4 v[104:107], v246, s[36:39], s91 offen offset:0 sc1\n"
    "s_nop 6\n"
    "v_mul_f32 v76, v76, v108\n"
    "v_mul_f32 v77, v77, v109\n"
    "v_mul_f32 v78, v78, v110\n"
    "v_mul_f32 v79, v79, v111\n"
    "v_mul_f32 v92, v92, v124\n"
    "v_mul_f32 v93, v93, v125\n"
    "v_mul_f32 v94, v94, v126\n"
    "v_mul_f32 v95, v95, v127\n"
    "v_cvt_pk_bf16_f32 v108, v76, v77\n"
    "v_cvt_pk_bf16_f32 v109, v78, v79\n"
    "v_cvt_pk_bf16_f32 v110, v92, v93\n"
    "v_cvt_pk_bf16_f32 v111, v94, v95\n"
    "buffer_store_dwordx4 v[108:111], v246, s[36:39], s91 offen offset:2048 sc1\n"
    "s_waitcnt lgkmcnt(0)\n"
    "s_xor_b64 s[64:65], s[64:65], s[44:45]\n"
    "s_xor_b64 s[66:67], s[66:67], s[44:45]\n"
    "s_xor_b64 s[68:69], s[68:69], s[44:45]\n"
    "s_xor_b64 s[70:71], s[70:71], s[44:45]\n"
    "s_xor_b64 s[72:73], s[72:73], s[44:45]\n"
    "s_xor_b64 s[74:75], s[74:75], s[44:45]\n"
    "s_xor_b64 s[76:77], s[76:77], s[44:45]\n"
    "s_or_b64 s[64:65], s[64:65], s[66:67]\n"
    "s_or_b64 s[64:65], s[64:65], s[68:69]\n"
    "s_or_b64 s[64:65], s[64:65], s[70:71]\n"
    "s_or_b64 s[64:65], s[64:65], s[72:73]\n"
    "s_or_b64 s[64:65], s[64:65], s[74:75]\n"
    "s_or_b64 s[64:65], s[64:65], s[76:77]\n"
    "s_cbranch_scc1 .Lg_slowA\n"
    "buffer_load_dwordx4 a[0:3], v246, s[36:39], s8 offen offset:0 sc1\n"
    "ds_read_b64_tr_b16 v[54:55], v219 offset:38912\n"
    "ds_read_b64_tr_b16 v[56:57], v220 offset:36864\n"
    "ds_read_b64_tr_b16 v[58:59], v220 offset:38912\n"
    "ds_read_b64_tr_b16 v[60:61], v221 offset:36864\n"
    "ds_read_b64_tr_b16 v[62:63], v221 offset:38912\n"
    "ds_read_b64_tr_b16 a[128:129], v218 offset:49152\n"
    "buffer_load_dwordx4 a[4:7], v246, s[36:39], s8 offen offset:2048 sc1\n"
    "ds_read_b64_tr_b16 a[130:131], v218 offset:51200\n"
    "ds_read_b64_tr_b16 a[132:133], v219 offset:49152\n"
    "ds_read_b64_tr_b16 a[134:135], v219 offset:51200\n"
    "ds_read_b64_tr_b16 a[136:137], v220 offset:49152\n"
    "ds_read_b64_tr_b16 a[138:139], v220 offset:51200\n"
    "ds_read_b64_tr_b16 a[140:141], v221 offset:49152\n"
    "buffer_load_dwordx4 a[8:11], v247, s[36:39], s8 offen offset:0 sc1\n"
    "ds_read_b64_tr_b16 a[142:143], v221 offset:51200\n"
    "ds_read_b64_tr_b16 a[144:145], v218 offset:53248\n"
    "ds_read_b64_tr_b16 a[146:147], v218 offset:55296\n"
    "ds_read_b64_tr_b16 a[148:149], v219 offset:53248\n"
    "ds_read_b64_tr_b16 a[150:151], v219 offset:55296\n"
    "ds_read_b64_tr_b16 a[152:153], v220 offset:53248\n"
    "buffer_load_dwordx4 a[12:15], v247, s[36:39], s8 offen offset:2048 sc1\n"
    "ds_read_b64_tr_b16 a[154:155], v220 offset:55296\n"
    "ds_read_b64_tr_b16 a[156:157], v221 offset:53248\n"
    "ds_read_b64_tr_b16 a[158:159], v221 offset:55296\n"
    "ds_read_b64_tr_b16 a[160:161], v214 offset:16384\n"
    "ds_read_b64_tr_b16 a[162:163], v214 offset:18432\n"
    "ds_read_b64_tr_b16 a[164:165], v215 offset:16384\n"
    "buffer_load_dwordx4 a[16:19], v246, s[36:39], s9 offen offset:0 sc1\n"
    "ds_read_b64_tr_b16 a[166:167], v215 offset:18432\n"
    "ds_read_b64_tr_b16 a[168:169], v216 offset:16384\n"
    "ds_read_b64_tr_b16 a[170:171], v216 offset:18432\n"
    "ds_read_b64_tr_b16 a[172:173], v217 offset:16384\n"
    "ds_read_b64_tr_b16 a[174:175], v217 offset:18432\n"
    "ds_read_b64_tr_b16 a[176:177], v214 offset:20480\n"
    "buffer_load_dwordx4 a[20:23], v246, s[36:39], s9 offen offset:2048 sc1\n"
    "ds_read_b64_tr_b16 a[178:179], v214 offset:22528\n"
    "ds_read_b64_tr_b16 a[180:181], v215 offset:20480\n"
    "ds_read_b64_tr_b16 a[182:183], v215 offset:22528\n"
    "ds_read_b64_tr_b16 a[184:185], v216 offset:20480\n"
    "ds_read_b64_tr_b16 a[186:187], v216 offset:22528\n"
    "ds_read_b64_tr_b16 a[188:189], v217 offset:20480\n"
    "buffer_load_dwordx4 a[24:27], v247, s[36:39], s9 offen offset:0 sc1\n"
    "ds_read_b64_tr_b16 a[190:191], v217 offset:22528\n"
    "ds_read_b64_tr_b16 v[64:65], v218 offset:16384\n"
    "buffer_load_dwordx4 a[28:31], v247, s[36:39], s9 offen offset:2048 sc1\n"
    "s_waitcnt lgkmcnt(0)\n"
    "s_waitcnt vmcnt(8)\n"
    "s_barrier\n"
    "s_cmp_lg_u32 s88, 0\n"
    "s_cbranch_scc1 .Lg_noflag_3\n"
    "s_add_u32 s89, s89, 524288\n"
    "buffer_store_dwordx4 v[228:231], v227, s[36:39], s89 offen sc1\n"
    ".Lg_noflag_3:\n"
    ".Lg_joinA:\n"
    "buffer_load_dwordx4 a[32:35], v246, s[36:39], s10 offen offset:0 sc1\n"
    "buffer_load_dwordx4 a[36:39], v246, s[36:39], s10 offen offset:2048 sc1\n"
    "buffer_load_dwordx4 a[40:43], v247, s[36:39], s10 offen offset:0 sc1\n"
    "buffer_load_dwordx4 a[44:47], v247, s[36:39], s10 offen offset:2048 sc1\n"
    "s_waitcnt vmcnt(10)\n"
    "v_mfma_f32_16x16x32_bf16 v[128:131], a[192:195], a[0:3], 0\n"
    "ds_read_b64_tr_b16 v[66:67], v218 offset:18432\n"
    "v_mfma_f32_16x16x32_bf16 v[132:135], a[192:195], a[4:7], 0\n"
    "buffer_load_dwordx4 a[48:51], v246, s[36:39], s11 offen offset:0 sc1\n"
    "v_mfma_f32_16x16x32_bf16 v[136:139], a[196:199], a[0:3], 0\n"
    "ds_read_b64_tr_b16 v[68:69], v219 offset:16384\n"
    "v_mfma_f32_16x16x32_bf16 v[140:143], a[196:199], a[4:7], 0\n"
    "ds_read_b64_tr_b16 v[70:71], v219 offset:18432\n"
    "v_mfma_f32_16x16x32_bf16 v[144:147], a[200:203], a[0:3], 0\n"
    "ds_read_b64_tr_b16 v[72:73], v220 offset:16384\n"
    "v_mfma_f32_16x16x32_bf16 v[148:151], a[200:203], a[4:7], 0\n"
    "buffer_load_dwordx4 a[52:55], v246, s[36:39], s11 offen offset:2048 sc1\n"
    "v_mfma_f32_16x16x32_bf16 v[152:155], a[204:207], a[0:3], 0\n"
    "ds_read_b64_tr_b16 v[74:75], v220 offset:18432\n"
    "v_mfma_f32_16x16x32_bf16 v[156:159], a[204:207], a[4:7], 0\n"
    "ds_read_b64_tr_b16 v[76:77], v221 offset:16384\n"
    "s_waitcnt vmcnt(10)\n"
    "v_mfma_f32_16x16x32_bf16 v[128:131], a[208:211], a[8:11], v[128:131]\n"
    "ds_read_b64_tr_b16 v[78:79], v221 offset:18432\n"
    "v_mfma_f32_16x16x32_bf16 v[132:135], a[208:211], a[12:15], v[132:135]\n"
    "buffer_load_dwordx4 a[56:59], v247, s[36:39], s11 offen offset:0 sc1\n"
    "v_mfma_f32_16x16x32_bf16 v[136:139], a[212:215], a[8:11], v[136:139]\n"
    "ds_read_b64_tr_b16 v[80:81], v218 offset:20480\n"
    "v_mfma_f32_16x16x32_bf16 v[140:143], a[212:215], a[12:15], v[140:143]\n"
    "ds_read_b64_tr_b16 v[82:83], v218 offset:22528\n"
    "v_mfma_f32_16x16x32_bf16 v[144:147], a[216:219], a[8:11], v[144:147]\n"
    "ds_read_b64_tr_b16 v[84:85], v219 offset:20480\n"
    "v_mfma_f32_16x16x32_bf16 v[148:151], a[216:219], a[12:15], v[148:151]\n"
    "buffer_load_dwordx4 a[60:63], v247, s[36:39], s11 offen offset:2048 sc1\n"
    "v_mfma_f32_16x16x32_bf16 v[152:155], a[220:223], a[8:11], v[152:155]\n"
    "ds_read_b64_tr_b16 v[86:87], v219 offset:22528\n"
    "s_add_u32 s98, s86, 65536\n"
    "v_mfma_f32_16x16x32_bf16 v[156:159], a[220:223], a[12:15], v[156:159]\n"
    "ds_read_b64_tr_b16 v[88:89], v220 offset:20480\n"
    "s_waitcnt vmcnt(10)\n"
    "v_mfma_f32_16x16x32_bf16 v[128:131], a[224:227], a[16:19], v[128:131]\n"
    "ds_read_b64_tr_b16 v[90:91], v220 offset:22528\n"
    "s_add_u32 m0, s63, 0\n"
    "v_mfma_f32_16x16x32_bf16 v[132:135], a[224:227], a[20:23], v[132:135]\n"
    "buffer_load_dwordx4 a[0:3], v246, s[36:39], s12 offen offset:0 sc1\n"
    "v_mfma_f32_16x16x32_bf16 v[136:139], a[228:231], a[16:19], v[136:139]\n"
    "buffer_load_dwordx4 v198, s[32:35], s98 offen offset:0 lds\n"
    "v_mfma_f32_16x16x32_bf16 v[140:143], a[228:231], a[20:23], v[140:143]\n"
    "ds_read_b64_tr_b16 v[92:93], v221 offset:20480\n"
    "v_mfma_f32_16x16x32_bf16 v[144:147], a[232:235], a[16:19], v[144:147]\n"
    "buffer_load_dwordx4 v199, s[32:35], s98 offen offset:1024 lds\n"
    "v_mfma_f32_16x16x32_bf16 v[148:151], a[232:235], a[20:23], v[148:151]\n"
    "buffer_load_dwordx4 a[4:7], v246, s[36:39], s12 offen offset:2048 sc1\n"
    "v_mfma_f32_16x16x32_bf16 v[152:155], a[236:239], a[16:19], v[152:155]\n"
    "ds_read_b64_tr_b16 v[94:95], v221 offset:22528\n"
    "v_mfma_f32_16x16x32_bf16 v[156:159], a[236:239], a[20:23], v[156:159]\n"
    "ds_read_b64_tr_b16 v[96:97], v218 offset:0\n"
    "s_waitcnt vmcnt(12)\n"
    "v_mfma_f32_16x16x32_bf16 v[128:131], a[240:243], a[24:27], v[128:131]\n"
    "buffer_load_dwordx4 v200, s[32:35], s98 offen offset:2048 lds\n"
    "v_mfma_f32_16x16x32_bf16 v[132:135], a[240:243], a[28:31], v[132:135]\n"
    "buffer_load_dwordx4 a[8:11], v247, s[36:39], s12 offen offset:0 sc1\n"
    "v_mfma_f32_16x16x32_bf16 v[136:139], a[244:247], a[24:27], v[136:139]\n"
    "ds_read_b64_tr_b16 v[98:99], v218 offset:2048\n"
    "v_mfma_f32_16x16x32_bf16 v[140:143], a[244:247], a[28:31], v[140:143]\n"
    "ds_read_b64_tr_b16 v[100:101], v219 offset:0\n"
    "v_mfma_f32_16x16x32_bf16 v[144:147], a[248:251], a[24:27], v[144:147]\n"
    "buffer_load_dwordx4 v201, s[32:35], s98 offen offset:3072 lds\n"
    "v_mfma_f32_16x16x32_bf16 v[148:151], a[248:251], a[28:31], v[148:151]\n"
    "buffer_load_dwordx4 a[12:15], v247, s[36:39], s12 offen offset:2048 sc1\n"
    "v_mfma_f32_16x16x32_bf16 v[152:155], a[252:255], a[24:27], v[152:155]\n"
    "ds_read_b64_tr_b16 v[102:103], v219 offset:2048\n"
    "v_mfma_f32_16x16x32_bf16 v[156:159], a[252:255], a[28:31], v[156:159]\n"
    "ds_read_b64_tr_b16 v[104:105], v220 offset:0\n"
    "s_waitcnt vmcnt(14)\n"
    "v_mfma_f32_16x16x32_bf16 v[128:131], a[64:67], a[32:35], v[128:131]\n"
    "ds_read_b64_tr_b16 v[106:107], v220 offset:2048\n"
    "v_mfma_f32_16x16x32_bf16 v[132:135], a[64:67], a[36:39], v[132:135]\n"
    "buffer_load_dwordx4 a[16:19], v246, s[36:39], s13 offen offset:0 sc1\n"
    "v_mfma_f32_16x16x32_bf16 v[136:139], a[68:71], a[32:35], v[136:139]\n"
    "ds_read_b64_tr_b16 v[108:109], v221 offset:0\n"
    "v_mfma_f32_16x16x32_bf16 v[140:143], a[68:71], a[36:39], v[140:143]\n"
    "ds_read_b64_tr_b16 v[110:111], v221 offset:2048\n"
    "v_mfma_f32_16x16x32_bf16 v[144:147], a[72:75], a[32:35], v[144:147]\n"
    "v_mfma_f32_16x16x32_bf16 v[148:151], a[72:75], a[36:39], v[148:151]\n"
    "buffer_load_dwordx4 a[20:23], v246, s[36:39], s13 offen offset:2048 sc1\n"
    "v_mfma_f32_16x16x32_bf16 v[152:155], a[76:79], a[32:35], v[152:155]\n"
    "v_mfma_f32_16x16x32_bf16 v[156:159], a[76:79], a[36:39], v[156:159]\n"
    "s_waitcnt vmcnt(14)\n"
    "v_mfma_f32_16x16x32_bf16 v[128:131], a[80:83], a[40:43], v[128:131]\n"
    "s_waitcnt lgkmcnt(0)\n"
    "s_load_dwordx2 s[64:65], s[96:97], 0x80000 glc\n"
    "s_load_dwordx2 s[66:67], s[96:97], 0x80100 glc\n"
    "s_load_dwordx2 s[68:69], s[96:97], 0x80200 glc\n"
    "s_load_dwordx2 s[70:71], s[96:97], 0x80300 glc\n"
    "s_load_dwordx2 s[72:73], s[96:97], 0x80400 glc\n"
    "s_load_dwordx2 s[74:75], s[96:97], 0x80500 glc\n"
    "s_load_dwordx2 s[76:77], s[96:97], 0x80600 glc\n"
    "v_mfma_f32_16x16x32_bf16 v[132:135], a[80:83], a[44:47], v[132:135]\n"
    "buffer_load_dwordx4 a[24:27], v247, s[36:39], s13 offen offset:0 sc1\n"
    "v_mfma_f32_16x16x32_bf16 v[136:139], a[84:87], a[40:43], v[136:139]\n"
    "v_mfma_f32_16x16x32_bf16 v[140:143], a[84:87], a[44:47], v[140:143]\n"
    "v_mfma_f32_16x16x32_bf16 v[144:147], a[88:91], a[40:43], v[144:147]\n"
    "v_mfma_f32_16x16x32_bf16 v[148:151], a[88:91], a[44:47], v[148:151]\n"
    "buffer_load_dwordx4 a[28:31], v247, s[36:39], s13 offen offset:2048 sc1\n"
    "v_mfma_f32_16x16x32_bf16 v[152:155], a[92:95], a[40:43], v[152:155]\n"
    "v_mfma_f32_16x16x32_bf16 v[156:159], a[92:95], a[44:47], v[156:159]\n"
    "s_waitcnt vmcnt(14)\n"
    "v_mfma_f32_16x16x32_bf16 v[128:131], v[0:3], a[48:51], v[128:131]\n"
    "v_mfma_f32_16x16x32_bf16 v[132:135], v[0:3], a[52:55], v[132:135]\n"
    "buffer_load_dwordx4 a[32:35], v246, s[36:39], s14 offen offset:0 sc1\n"
    "v_mfma_f32_16x16x32_bf16 v[136:139], v[4:7], a[48:51], v[136:139]\n"
    "v_mfma_f32_16x16x32_bf16 v[140:143], v[4:7], a[52:55], v[140:143]\n"
    "v_mfma_f32_16x16x32_bf16 v[144:147], v[8:11], a[48:51], v[144:147]\n"
    "v_mfma_f32_16x16x32_bf16 v[148:151], v[8:11], a[52:55], v[148:151]\n"
    "buffer_load_dwordx4 a[36:39], v246, s[36:39], s14 offen offset:2048 sc1\n"
    "v_mfma_f32_16x16x32_bf16 v[152:155], v[12:15], a[48:51], v[152:155]\n"
    "v_mfma_f32_16x16x32_bf16 v[156:159], v[12:15], a[52:55], v[156:159]\n"
    "s_waitcnt vmcnt(14)\n"
    "v_mfma_f32_16x16x32_bf16 v[128:131], v[16:19], a[56:59], v[128:131]\n"
    "v_mfma_f32_16x16x32_bf16 v[132:135], v[16:19], a[60:63], v[132:135]\n"
    "buffer_load_dwordx4 a[40:43], v247, s[36:39], s14 offen offset:0 sc1\n"
    "v_mfma_f32_16x16x32_bf16 v[136:139], v[20:23], a[56:59], v[136:139]\n"
    "v_mfma_f32_16x16x32_bf16 v[140:143], v[20:23], a[60:63], v[140:143]\n"
    "v_mfma_f32_16x16x32_bf16 v[144:147], v[24:27], a[56:59], v[144:147]\n"
    "v_mfma_f32_16x16x32_bf16 v[148:151], v[24:27], a[60:63], v[148:151]\n"
    "buffer_load_dwordx4 a[44:47], v247, s[36:39], s14 offen offset:2048 sc1\n"
    "v_mfma_f32_16x16x32_bf16 v[152:155], v[28:31], a[56:59], v[152:155]\n"
    "v_mfma_f32_16x16x32_bf16 v[156:159], v[28:31], a[60:63], v[156:159]\n"
    "s_waitcnt vmcnt(12)\n"
    "v_mfma_f32_16x16x32_bf16 v[128:131], a[96:99], a[0:3], v[128:131]\n"
    "s_waitcnt lgkmcnt(0)\n"
    "s_xor_b64 s[64:65], s[64:65], s[44:45]\n"
    "s_xor_b64 s[66:67], s[66:67], s[44:45]\n"
    "s_xor_b64 s[68:69], s[68:69], s[44:45]\n"
    "s_xor_b64 s[70:71], s[70:71], s[44:45]\n"
    "s_xor_b64 s[72:73], s[72:73], s[44:45]\n"
    "s_xor_b64 s[74:75], s[74:75], s[44:45]\n"
    "s_xor_b64 s[76:77], s[76:77], s[44:45]\n"
    "s_or_b64 s[64:65], s[64:65], s[66:67]\n"
    "s_or_b64 s[64:65], s[64:65], s[68:69]\n"
    "s_or_b64 s[64:65], s[64:65], s[70:71]\n"
    "s_or_b64 s[64:65], s[64:65], s[72:73]\n"
    "s_or_b64 s[64:65], s[64:65], s[74:75]\n"
    "s_or_b64 s[64:65], s[64:65], s[76:77]\n"
    "s_cbranch_scc1 .Lg_slowB\n"
    ".Lg_okB:\n"
    "v_mfma_f32_16x16x32_bf16 v[132:135], a[96:99], a[4:7], v[132:135]\n"
    "s_add_u32 s8, s8, 8192\n"
    "buffer_load_dwordx4 a[48:51], v246, s[36:39], s8 offen offset:0 sc1\n"
    "v_mfma_f32_16x16x32_bf16 v[136:139], a[100:103], a[0:3], v[136:139]\n"
    "ds_read_b64_tr_b16 v[112:113], v218 offset:4096\n"
    "v_mfma_f32_16x16x32_bf16 v[140:143], a[100:103], a[4:7], v[140:143]\n"
    "ds_read_b64_tr_b16 v[114:115], v218 offset:6144\n"
    "v_mfma_f32_16x16x32_bf16 v[144:147], a[104:107], a[0:3], v[144:147]\n"
    "ds_read_b64_tr_b16 v[116:117], v219 offset:4096\n"
    "v_mfma_f32_16x16x32_bf16 v[148:151], a[104:107], a[4:7], v[148:151]\n"
    "buffer_load_dwordx4 a[52:55], v246, s[36:39], s8 offen offset:2048 sc1\n"
    "v_mfma_f32_16x16x32_bf16 v[152:155], a[108:111], a[0:3], v[152:155]\n"
    "ds_read_b64_tr_b16 v[118:119], v219 offset:6144\n"
    "v_mfma_f32_16x16x32_bf16 v[156:159], a[108:111], a[4:7], v[156:159]\n"
    "ds_read_b64_tr_b16 v[120:121], v220 offset:4096\n"
    "s_waitcnt vmcnt(10)\n"
    "v_mfma_f32_16x16x32_bf16 v[128:131], a[112:115], a[8:11], v[128:131]\n"
    "ds_read_b64_tr_b16 v[122:123], v220 offset:6144\n"
    "v_mfma_f32_16x16x32_bf16 v[132:135], a[112:115], a[12:15], v[132:135]\n"
    "buffer_load_dwordx4 a[56:59], v247, s[36:39], s8 offen offset:0 sc1\n"
    "v_mfma_f32_16x16x32_bf16 v[136:139], a[116:119], a[8:11], v[136:139]\n"
    "ds_read_b64_tr_b16 v[124:125], v221 offset:4096\n"
    "v_mfma_f32_16x16x32_bf16 v[140:143], a[116:119], a[12:15], v[140:143]\n"
    "ds_read_b64_tr_b16 v[126:127], v221 offset:6144\n"
    "v_mfma_f32_16x16x32_bf16 v[144:147], a[120:123], a[8:11], v[144:147]\n"
    "ds_read_b64_tr_b16 v[160:161], v214 offset:32768\n"
    "v_mfma_f32_16x16x32_bf16 v[148:151], a[120:123], a[12:15], v[148:151]\n"
    "buffer_load_dwordx4 a[60:63], v247, s[36:39], s8 offen offset:2048 sc1\n"
    "v_mfma_f32_16x16x32_bf16 v[152:155], a[124:127], a[8:11], v[152:155]\n"
    "ds_read_b64_tr_b16 v[162:163], v214 offset:34816\n"
    "v_mfma_f32_16x16x32_bf16 v[156:159], a[124:127], a[12:15], v[156:159]\n"
    "ds_read_b64_tr_b16 v[164:165], v215 offset:32768\n"
    "s_waitcnt vmcnt(10)\n"
    "v_mfma_f32_16x16x32_bf16 v[128:131], v[32:35], a[16:19], v[128:131]\n"
    "s_barrier\n"
    "v_mfma_f32_16x16x32_bf16 v[132:135], v[32:35], a[20:23], v[132:135]\n"
    "s_add_u32 s9, s9, 8192\n"
    "buffer_load_dwordx4 a[0:3], v246, s[36:39], s9 offen offset:0 sc1\n"
    "v_mfma_f32_16x16x32_bf16 v[136:139], v[36:39], a[16:19], v[136:139]\n"
    "ds_read_b64_tr_b16 v[166:167], v215 offset:34816\n"
    "v_mfma_f32_16x16x32_bf16 v[140:143], v[36:39], a[20:23], v[140:143]\n"
    "ds_read_b64_tr_b16 v[168:169], v216 offset:32768\n"
    "v_mfma_f32_16x16x32_bf16 v[144:147], v[40:43], a[16:19], v[144:147]\n"
    "ds_read_b64_tr_b16 v[170:171], v216 offset:34816\n"
    "v_mfma_f32_16x16x32_bf16 v[148:151], v[40:43], a[20:23], v[148:151]\n"
    "buffer_load_dwordx4 a[4:7], v246, s[36:39], s9 offen offset:2048 sc1\n"
    "v_mfma_f32_16x16x32_bf16 v[152:155], v[44:47], a[16:19], v[152:155]\n"
    "ds_read_b64_tr_b16 v[172:173], v217 offset:32768\n"
    "v_mfma_f32_16x16x32_bf16 v[156:159], v[44:47], a[20:23], v[156:159]\n"
    "ds_read_b64_tr_b16 v[174:175], v217 offset:34816\n"
    "s_waitcnt vmcnt(10)\n"
    "v_mfma_f32_16x16x32_bf16 v[128:131], v[48:51], a[24:27], v[128:131]\n"
    "ds_read_b64_tr_b16 v[176:177], v214 offset:36864\n"
    "v_mfma_f32_16x16x32_bf16 v[132:135], v[48:51], a[28:31], v[132:135]\n"
    "buffer_load_dwordx4 a[8:11], v247, s[36:39], s9 offen offset:0 sc1\n"
    "v_mfma_f32_16x16x32_bf16 v[136:139], v[52:55], a[24:27], v[136:139]\n"
    "ds_read_b64_tr_b16 v[178:179], v214 offset:38912\n"
    "v_mfma_f32_16x16x32_bf16 v[140:143], v[52:55], a[28:31], v[140:143]\n"
    "ds_read_b64_tr_b16 v[180:181], v215 offset:36864\n"
    "v_mfma_f32_16x16x32_bf16 v[144:147], v[56:59], a[24:27], v[144:147]\n"
    "ds_read_b64_tr_b16 v[182:183], v215 offset:38912\n"
    "v_mfma_f32_16x16x32_bf16 v[148:151], v[56:59], a[28:31], v[148:151]\n"
    "buffer_load_dwordx4 a[12:15], v247, s[36:39], s9 offen offset:2048 sc1\n"
    "v_mfma_f32_16x16x32_bf16 v[152:155], v[60:63], a[24:27], v[152:155]\n"
    "ds_read_b64_tr_b16 v[184:185], v216 offset:36864\n"
    "v_mfma_f32_16x16x32_bf16 v[156:159], v[60:63], a[28:31], v[156:159]\n"
    "ds_read_b64_tr_b16 v[186:187], v216 offset:38912\n"
    "s_waitcnt vmcnt(10)\n"
    "v_mfma_f32_16x16x32_bf16 v[128:131], a[128:131], a[32:35], v[128:131]\n"
    "ds_read_b64_tr_b16 v[188:189], v217 offset:36864\n"
    "v_mfma_f32_16x16x32_bf16 v[132:135], a[128:131], a[36:39], v[132:135]\n"
    "s_add_u32 s10, s10, 8192\n"
    "buffer_load_dwordx4 a[16:19], v246, s[36:39], s10 offen offset:0 sc1\n"
    "v_mfma_f32_16x16x32_bf16 v[136:139], a[132:135], a[32:35], v[136:139]\n"
    "ds_read_b64_tr_b16 v[190:191], v217 offset:38912\n"
    "v_mfma_f32_16x16x32_bf16 v[140:143], a[132:135], a[36:39], v[140:143]\n"
    "ds_read_b64_tr_b16 a[192:193], v214 offset:49152\n"
    "v_mfma_f32_16x16x32_bf16 v[144:147], a[136:139], a[32:35], v[144:147]\n"
    "ds_read_b64_tr_b16 a[194:195], v214 offset:51200\n"
    "v_mfma_f32_16x16x32_bf16 v[148:151], a[136:139], a[36:39], v[148:151]\n"
    "buffer_load_dwordx4 a[20:23], v246, s[36:39], s10 offen offset:2048 sc1\n"
    "v_mfma_f32_16x16x32_bf16 v[152:155], a[140:143], a[32:35], v[152:155]\n"
    "ds_read_b64_tr_b16 a[196:197], v215 offset:49152\n"
    "v_mfma_f32_16x16x32_bf16 v[156:159], a[140:143], a[36:39], v[156:159]\n"
    "ds_read_b64_tr_b16 a[198:199], v215 offset:51200\n"
    "s_waitcnt vmcnt(10)\n"
    "v_mfma_f32_16x16x32_bf16 v[128:131], a[144:147], a[40:43], v[128:131]\n"
    "ds_read_b64_tr_b16 a[200:201], v216 offset:49152\n"
    "v_mfma_f32_16x16x32_bf16 v[132:135], a[144:147], a[44:47], v[132:135]\n"
    "buffer_load_dwordx4 a[24:27], v247, s[36:39], s10 offen offset:0 sc1\n"
    "v_mfma_f32_16x16x32_bf16 v[136:139], a[148:151], a[40:43], v[136:139]\n"
    "ds_read_b64_tr_b16 a[202:203], v216 offset:51200\n"
    "v_mfma_f32_16x16x32_bf16 v[140:143], a[148:151], a[44:47], v[140:143]\n"
    "ds_read_b64_tr_b16 a[204:205], v217 offset:49152\n"
    "v_mfma_f32_16x16x32_bf16 v[144:147], a[152:155], a[40:43], v[144:147]\n"
    "ds_read_b64_tr_b16 a[206:207], v217 offset:51200\n"
    "v_mfma_f32_16x16x32_bf16 v[148:151], a[152:155], a[44:47], v[148:151]\n"
    "buffer_load_dwordx4 a[28:31], v247, s[36:39], s10 offen offset:2048 sc1\n"
    "v_mfma_f32_16x16x32_bf16 v[152:155], a[156:159], a[40:43], v[152:155]\n"
    "ds_read_b64_tr_b16 a[208:209], v214 offset:53248\n"
    "v_mfma_f32_16x16x32_bf16 v[156:159], a[156:159], a[44:47], v[156:159]\n"
    "ds_read_b64_tr_b16 a[210:211], v214 offset:55296\n"
    "s_waitcnt vmcnt(10)\n"
    "v_mfma_f32_16x16x32_bf16 v[128:131], a[160:163], a[48:51], v[128:131]\n"
    "ds_read_b64_tr_b16 a[212:213], v215 offset:53248\n"
    "v_mfma_f32_16x16x32_bf16 v[132:135], a[160:163], a[52:55], v[132:135]\n"
    "s_add_u32 s11, s11, 8192\n"
    "buffer_load_dwordx4 a[32:35], v246, s[36:39], s11 offen offset:0 sc1\n"
    "v_mfma_f32_16x16x32_bf16 v[136:139], a[164:167], a[48:51], v[136:139]\n"
    "ds_read_b64_tr_b16 a[214:215], v215 offset:55296\n"
    "v_mfma_f32_16x16x32_bf16 v[140:143], a[164:167], a[52:55], v[140:143]\n"
    "ds_read_b64_tr_b16 a[216:217], v216 offset:53248\n"
    "v_mfma_f32_16x16x32_bf16 v[144:147], a[168:171], a[48:51], v[144:147]\n"
    "ds_read_b64_tr_b16 a[218:219], v216 offset:55296\n"
    "v_mfma_f32_16x16x32_bf16 v[148:151], a[168:171], a[52:55], v[148:151]\n"
    "buffer_load_dwordx4 a[36:39], v246, s[36:39], s11 offen offset:2048 sc1\n"
    "v_mfma_f32_16x16x32_bf16 v[152:155], a[172:175], a[48:51], v[152:155]\n"
    "ds_read_b64_tr_b16 a[220:221], v217 offset:53248\n"
    "v_mfma_f32_16x16x32_bf16 v[156:159], a[172:175], a[52:55], v[156:159]\n"
    "ds_read_b64_tr_b16 a[222:223], v217 offset:55296\n"
    "s_waitcnt vmcnt(10)\n"
    "v_mfma_f32_16x16x32_bf16 v[128:131], a[176:179], a[56:59], v[128:131]\n"
    "ds_read_b64_tr_b16 a[224:225], v222 offset:0\n"
    "v_mfma_f32_16x16x32_bf16 v[132:135], a[176:179], a[60:63], v[132:135]\n"
    "buffer_load_dwordx4 a[40:43], v247, s[36:39], s11 offen offset:0 sc1\n"
    "v_mfma_f32_16x16x32_bf16 v[136:139], a[180:183], a[56:59], v[136:139]\n"
    "ds_read_b64_tr_b16 a[226:227], v222 offset:2048\n"
    "v_mfma_f32_16x16x32_bf16 v[140:143], a[180:183], a[60:63], v[140:143]\n"
    "ds_read_b64_tr_b16 a[228:229], v223 offset:0\n"
    "v_mfma_f32_16x16x32_bf16 v[144:147], a[184:187], a[56:59], v[144:147]\n"
    "ds_read_b64_tr_b16 a[230:231], v223 offset:2048\n"
    "v_mfma_f32_16x16x32_bf16 v[148:151], a[184:187], a[60:63], v[148:151]\n"
    "buffer_load_dwordx4 a[44:47], v247, s[36:39], s11 offen offset:2048 sc1\n"
    "v_mfma_f32_16x16x32_bf16 v[152:155], a[188:191], a[56:59], v[152:155]\n"
    "ds_read_b64_tr_b16 a[232:233], v224 offset:0\n"
    "v_mfma_f32_16x16x32_bf16 v[156:159], a[188:191], a[60:63], v[156:159]\n"
    "ds_read_b64_tr_b16 a[234:235], v224 offset:2048\n"
    "s_waitcnt vmcnt(10)\n"
    "v_mfma_f32_16x16x32_bf16 v[128:131], v[64:67], a[0:3], v[128:131]\n"
    "ds_read_b64_tr_b16 a[236:237], v225 offset:0\n"
    "v_mfma_f32_16x16x32_bf16 v[132:135], v[64:67], a[4:7], v[132:135]\n"
    "s_add_u32 s12, s12, 8192\n"
    "buffer_load_dwordx4 a[48:51], v246, s[36:39], s12 offen offset:0 sc1\n"
    "v_mfma_f32_16x16x32_bf16 v[136:139], v[68:71], a[0:3], v[136:139]\n"
    "ds_read_b64_tr_b16 a[238:239], v225 offset:2048\n"
    "v_mfma_f32_16x16x32_bf16 v[140:143], v[68:71], a[4:7], v[140:143]\n"
    "ds_read_b64_tr_b16 a[240:241], v222 offset:4096\n"
    "v_mfma_f32_16x16x32_bf16 v[144:147], v[72:75], a[0:3], v[144:147]\n"
    "ds_read_b64_tr_b16 a[242:243], v222 offset:6144\n"
    "v_mfma_f32_16x16x32_bf16 v[148:151], v[72:75], a[4:7], v[148:151]\n"
    "buffer_load_dwordx4 a[52:55], v246, s[36:39], s12 offen offset:2048 sc1\n"
    "v_mfma_f32_16x16x32_bf16 v[152:155], v[76:79], a[0:3], v[152:155]\n"
    "ds_read_b64_tr_b16 a[244:245], v223 offset:4096\n"
    "v_mfma_f32_16x16x32_bf16 v[156:159], v[76:79], a[4:7], v[156:159]\n"
    "ds_read_b64_tr_b16 a[246:247], v223 offset:6144\n"
    "s_waitcnt vmcnt(10)\n"
    "v_mfma_f32_16x16x32_bf16 v[128:131], v[80:83], a[8:11], v[128:131]\n"
    "ds_read_b64_tr_b16 a[248:249], v224 offset:4096\n"
    "v_mfma_f32_16x16x32_bf16 v[132:135], v[80:83], a[12:15], v[132:135]\n"
    "buffer_load_dwordx4 a[56:59], v247, s[36:39], s12 offen offset:0 sc1\n"
    "v_mfma_f32_16x16x32_bf16 v[136:139], v[84:87], a[8:11], v[136:139]\n"
    "ds_read_b64_tr_b16 a[250:251], v224 offset:6144\n"
    "v_mfma_f32_16x16x32_bf16 v[140:143], v[84:87], a[12:15], v[140:143]\n"
    "ds_read_b64_tr_b16 a[252:253], v225 offset:4096\n"
    "v_mfma_f32_16x16x32_bf16 v[144:147], v[88:91], a[8:11], v[144:147]\n"
    "ds_read_b64_tr_b16 a[254:255], v225 offset:6144\n"
    "v_mfma_f32_16x16x32_bf16 v[148:151], v[88:91], a[12:15], v[148:151]\n"
    "buffer_load_dwordx4 a[60:63], v247, s[36:39], s12 offen offset:2048 sc1\n"
    "v_mfma_f32_16x16x32_bf16 v[152:155], v[92:95], a[8:11], v[152:155]\n"
    "ds_read_b64_tr_b16 a[72:73], v216 offset:0\n"
    "v_mfma_f32_16x16x32_bf16 v[156:159], v[92:95], a[12:15], v[156:159]\n"
    "ds_read_b64_tr_b16 a[74:75], v216 offset:2048\n"
    "s_waitcnt vmcnt(10)\n"
    "v_mfma_f32_16x16x32_bf16 v[128:131], v[96:99], a[16:19], v[128:131]\n"
    "ds_read_b64_tr_b16 a[76:77], v217 offset:0\n"
    "v_mfma_f32_16x16x32_bf16 v[132:135], v[96:99], a[20:23], v[132:135]\n"
    "s_add_u32 s13, s13, 8192\n"
    "buffer_load_dwordx4 a[0:3], v246, s[36:39], s13 offen offset:0 sc1\n"
    "v_mfma_f32_16x16x32_bf16 v[136:139], v[100:103], a[16:19], v[136:139]\n"
    "ds_read_b64_tr_b16 a[78:79], v217 offset:2048\n"
    "v_mfma_f32_16x16x32_bf16 v[140:143], v[100:103], a[20:23], v[140:143]\n"
    "ds_read_b64_tr_b16 a[88:89], v216 offset:4096\n"
    "v_mfma_f32_16x16x32_bf16 v[144:147], v[104:107], a[16:19], v[144:147]\n"
    "ds_read_b64_tr_b16 a[90:91], v216 offset:6144\n"
    "v_mfma_f32_16x16x32_bf16 v[148:151], v[104:107], a[20:23], v[148:151]\n"
    "buffer_load_dwordx4 a[4:7], v246, s[36:39], s13 offen offset:2048 sc1\n"
    "v_mfma_f32_16x16x32_bf16 v[152:155], v[108:111], a[16:19], v[152:155]\n"
    "v_add_u32 v238, s63, v226\n"
    "v_add_u32 v238, 147456, v238\n"
    "v_mfma_f32_16x16x32_bf16 v[156:159], v[108:111], a[20:23], v[156:159]\n"
    "v_xor_b32 v239, 4096, v238\n"
    "s_lshl1_add_u32 s57, s54, s93\n"
    "s_waitcnt vmcnt(10) lgkmcnt(1)\n"
    "v_mfma_f32_16x16x32_bf16 v[128:131], v[112:115], a[24:27], v[128:131]\n"
    "ds_read_b64_tr_b16 a[92:93], v217 offset:4096\n"
    "s_lshl_b32 s57, s57, 15\n"
    "v_mfma_f32_16x16x32_bf16 v[132:135], v[112:115], a[28:31], v[132:135]\n"
    "buffer_load_dwordx4 a[8:11], v247, s[36:39], s13 offen offset:0 sc1\n"
    "s_lshl1_add_u32 s58, s55, s92\n"
    "v_mfma_f32_16x16x32_bf16 v[136:139], v[116:119], a[24:27], v[136:139]\n"
    "ds_read_b64_tr_b16 a[94:95], v217 offset:6144\n"
    "s_lshl_b32 s58, s58, 6\n"
    "v_mfma_f32_16x16x32_bf16 v[140:143], v[116:119], a[28:31], v[140:143]\n"
    "ds_read_b64_tr_b16 a[64:65], v214 offset:0\n"
    "s_add_u32 s57, s57, s58\n"
    "v_mfma_f32_16x16x32_bf16 v[144:147], v[120:123], a[24:27], v[144:147]\n"
    "ds_read_b64_tr_b16 a[66:67], v214 offset:2048\n"
    "s_add_u32 s58, s57, 16384\n"
    "v_mfma_f32_16x16x32_bf16 v[148:151], v[120:123], a[28:31], v[148:151]\n"
    "buffer_load_dwordx4 a[12:15], v247, s[36:39], s13 offen offset:2048 sc1\n"
    "v_and_b32 v240, 15, v232\n"
    "v_mfma_f32_16x16x32_bf16 v[152:155], v[124:127], a[24:27], v[152:155]\n"
    "v_lshlrev_b32 v240, 10, v240\n"
    "v_bfe_u32 v241, v232, 4, 1\n"
    "v_mfma_f32_16x16x32_bf16 v[156:159], v[124:127], a[28:31], v[156:159]\n"
    "v_lshl_add_u32 v240, v241, 5, v240\n"
    "v_lshrrev_b32 v241, 5, v232\n"
    "s_waitcnt vmcnt(10)\n"
    "v_mfma_f32_16x16x32_bf16 v[128:131], v[160:163], a[32:35], v[128:131]\n"
    "v_lshl_add_u32 v240, v241, 4, v240\n"
    "v_mfma_f32_16x16x32_bf16 v[132:135], v[160:163], a[36:39], v[132:135]\n"
    "s_add_u32 s14, s14, 8192\n"
    "buffer_load_dwordx4 a[16:19], v246, s[36:39], s14 offen offset:0 sc1\n"
    "v_mfma_f32_16x16x32_bf16 v[136:139], v[164:167], a[32:35], v[136:139]\n"
    "ds_read_b64_tr_b16 a[68:69], v215 offset:0\n"
    "v_mfma_f32_16x16x32_bf16 v[140:143], v[164:167], a[36:39], v[140:143]\n"
    "ds_read_b64_tr_b16 a[70:71], v215 offset:2048\n"
    "v_mfma_f32_16x16x32_bf16 v[144:147], v[168:171], a[32:35], v[144:147]\n"
    "ds_read_b64_tr_b16 a[80:81], v214 offset:4096\n"
    "v_mfma_f32_16x16x32_bf16 v[148:151], v[168:171], a[36:39], v[148:151]\n"
    "buffer_load_dwordx4 a[20:23], v246, s[36:39], s14 offen offset:2048 sc1\n"
    "v_mfma_f32_16x16x32_bf16 v[152:155], v[172:175], a[32:35], v[152:155]\n"
    "ds_read_b64_tr_b16 a[82:83], v214 offset:6144\n"
    "v_mfma_f32_16x16x32_bf16 v[156:159], v[172:175], a[36:39], v[156:159]\n"
    "ds_read_b64_tr_b16 a[84:85], v215 offset:4096\n"
    "s_waitcnt vmcnt(10)\n"
    "v_mfma_f32_16x16x32_bf16 v[128:131], v[176:179], a[40:43], v[128:131]\n"
    "ds_read_b64_tr_b16 a[86:87], v215 offset:6144\n"
    "v_mfma_f32_16x16x32_bf16 v[132:135], v[176:179], a[44:47], v[132:135]\n"
    "buffer_load_dwordx4 a[24:27], v247, s[36:39], s14 offen offset:0 sc1\n"
    "v_mfma_f32_16x16x32_bf16 v[136:139], v[180:183], a[40:43], v[136:139]\n"
    "v_mfma_f32_16x16x32_bf16 v[140:143], v[180:183], a[44:47], v[140:143]\n"
    "v_mfma_f32_16x16x32_bf16 v[144:147], v[184:187], a[40:43], v[144:147]\n"
    "v_mfma_f32_16x16x32_bf16 v[148:151], v[184:187], a[44:47], v[148:151]\n"
    "buffer_load_dwordx4 a[28:31], v247, s[36:39], s14 offen offset:2048 sc1\n"
    "v_mfma_f32_16x16x32_bf16 v[152:155], v[188:191], a[40:43], v[152:155]\n"
    "v_mfma_f32_16x16x32_bf16 v[156:159], v[188:191], a[44:47], v[156:159]\n"
    "s_waitcnt vmcnt(10)\n"
    "v_mfma_f32_16x16x32_bf16 v[128:131], a[192:195], a[48:51], v[128:131]\n"
    "v_mfma_f32_16x16x32_bf16 v[132:135], a[192:195], a[52:55], v[132:135]\n"
    "v_mfma_f32_16x16x32_bf16 v[136:139], a[196:199], a[48:51], v[136:139]\n"
    "v_mfma_f32_16x16x32_bf16 v[140:143], a[196:199], a[52:55], v[140:143]\n"
    "v_mfma_f32_16x16x32_bf16 v[144:147], a[200:203], a[48:51], v[144:147]\n"
    "v_mfma_f32_16x16x32_bf16 v[148:151], a[200:203], a[52:55], v[148:151]\n"
    "v_mfma_f32_16x16x32_bf16 v[152:155], a[204:207], a[48:51], v[152:155]\n"
    "v_mfma_f32_16x16x32_bf16 v[156:159], a[204:207], a[52:55], v[156:159]\n"
    "s_waitcnt vmcnt(8)\n"
    "v_mfma_f32_16x16x32_bf16 v[128:131], a[208:211], a[56:59], v[128:131]\n"
    "v_mfma_f32_16x16x32_bf16 v[132:135], a[208:211], a[60:63], v[132:135]\n"
    "v_mfma_f32_16x16x32_bf16 v[136:139], a[212:215], a[56:59], v[136:139]\n"
    "v_mfma_f32_16x16x32_bf16 v[140:143], a[212:215], a[60:63], v[140:143]\n"
    "v_mfma_f32_16x16x32_bf16 v[144:147], a[216:219], a[56:59], v[144:147]\n"
    "v_mfma_f32_16x16x32_bf16 v[148:151], a[216:219], a[60:63], v[148:151]\n"
    "v_mfma_f32_16x16x32_bf16 v[152:155], a[220:223], a[56:59], v[152:155]\n"
    "v_mfma_f32_16x16x32_bf16 v[156:159], a[220:223], a[60:63], v[156:159]\n"
    "s_waitcnt vmcnt(6)\n"
    "v_mfma_f32_16x16x32_bf16 v[128:131], a[224:227], a[0:3], v[128:131]\n"
    "v_mfma_f32_16x16x32_bf16 v[132:135], a[224:227], a[4:7], v[132:135]\n"
    "v_mfma_f32_16x16x32_bf16 v[136:139], a[228:231], a[0:3], v[136:139]\n"
    "v_mfma_f32_16x16x32_bf16 v[140:143], a[228:231], a[4:7], v[140:143]\n"
    "v_mfma_f32_16x16x32_bf16 v[144:147], a[232:235], a[0:3], v[144:147]\n"
    "v_mfma_f32_16x16x32_bf16 v[148:151], a[232:235], a[4:7], v[148:151]\n"
    "v_mfma_f32_16x16x32_bf16 v[152:155], a[236:239], a[0:3], v[152:155]\n"
    "v_mfma_f32_16x16x32_bf16 v[156:159], a[236:239], a[4:7], v[156:159]\n"
    "s_waitcnt vmcnt(4)\n"
    "v_mfma_f32_16x16x32_bf16 v[128:131], a[240:243], a[8:11], v[128:131]\n"
    "v_mfma_f32_16x16x32_bf16 v[132:135], a[240:243], a[12:15], v[132:135]\n"
    "v_mfma_f32_16x16x32_bf16 v[136:139], a[244:247], a[8:11], v[136:139]\n"
    "v_mfma_f32_16x16x32_bf16 v[140:143], a[244:247], a[12:15], v[140:143]\n"
    "v_mfma_f32_16x16x32_bf16 v[144:147], a[248:251], a[8:11], v[144:147]\n"
    "v_mfma_f32_16x16x32_bf16 v[148:151], a[248:251], a[12:15], v[148:151]\n"
    "v_mfma_f32_16x16x32_bf16 v[152:155], a[252:255], a[8:11], v[152:155]\n"
    "v_mfma_f32_16x16x32_bf16 v[156:159], a[252:255], a[12:15], v[156:159]\n"
    "s_waitcnt vmcnt(2)\n"
    "v_mfma_f32_16x16x32_bf16 v[144:147], a[72:75], a[16:19], v[144:147]\n"
    "v_mfma_f32_16x16x32_bf16 v[148:151], a[72:75], a[20:23], v[148:151]\n"
    "v_mfma_f32_16x16x32_bf16 v[152:155], a[76:79], a[16:19], v[152:155]\n"
    "v_mfma_f32_16x16x32_bf16 v[156:159], a[76:79], a[20:23], v[156:159]\n"
    "s_waitcnt vmcnt(0) lgkmcnt(0)\n"
    "v_mfma_f32_16x16x32_bf16 v[144:147], a[88:91], a[24:27], v[144:147]\n"
    "s_nop 2\n"
    "v_mfma_f32_16x16x32_bf16 v[148:151], a[88:91], a[28:31], v[148:151]\n"
    "s_nop 2\n"
    "v_mfma_f32_16x16x32_bf16 v[152:155], a[92:95], a[24:27], v[152:155]\n"
    "ds_write_b128 v238, v[144:147] offset:0\n"
    "v_mfma_f32_16x16x32_bf16 v[156:159], a[92:95], a[28:31], v[156:159]\n"
    "s_nop 2\n"
    "v_mfma_f32_16x16x32_bf16 v[128:131], a[64:67], a[16:19], v[128:131]\n"
    "ds_write_b128 v238, v[148:151] offset:1024\n"
    "v_mfma_f32_16x16x32_bf16 v[132:135], a[64:67], a[20:23], v[132:135]\n"
    "ds_write_b128 v238, v[152:155] offset:2048\n"
    "v_mfma_f32_16x16x32_bf16 v[136:139], a[68:71], a[16:19], v[136:139]\n"
    "ds_write_b128 v238, v[156:159] offset:3072\n"
    "v_mfma_f32_16x16x32_bf16 v[140:143], a[68:71], a[20:23], v[140:143]\n"
    "v_mfma_f32_16x16x32_bf16 v[128:131], a[80:83], a[24:27], v[128:131]\n"
    "v_mfma_f32_16x16x32_bf16 v[132:135], a[80:83], a[28:31], v[132:135]\n"
    "v_mfma_f32_16x16x32_bf16 v[136:139], a[84:87], a[24:27], v[136:139]\n"
    "v_mfma_f32_16x16x32_bf16 v[140:143], a[84:87], a[28:31], v[140:143]\n"
    "s_waitcnt lgkmcnt(0)\n"
    "s_barrier\n"
    "ds_read_b128 v[144:147], v239 offset:0\n"
    "ds_read_b128 v[152:155], v239 offset:2048\n"
    "ds_read_b128 v[148:151], v239 offset:1024\n"
    "ds_read_b128 v[156:159], v239 offset:3072\n"
    "s_waitcnt lgkmcnt(3)\n"
    "v_add_f32 v128, v128, v144\n"
    "v_add_f32 v129, v129, v145\n"
    "v_add_f32 v130, v130, v146\n"
    "v_add_f32 v131, v131, v147\n"
    "v_cvt_pk_bf16_f32 v192, v128, v129\n"
    "v_cvt_pk_bf16_f32 v193, v130, v131\n"
    "s_waitcnt lgkmcnt(2)\n"
    "v_add_f32 v136, v136, v152\n"
    "v_add_f32 v137, v137, v153\n"
    "v_add_f32 v138, v138, v154\n"
    "v_add_f32 v139, v139, v155\n"
    "v_cvt_pk_bf16_f32 v194, v136, v137\n"
    "v_cvt_pk_bf16_f32 v195, v138, v139\n"
    "s_nop 1\n"
    "v_permlane16_swap_b32 v192, v194\n"
    "v_permlane16_swap_b32 v193, v195\n"
    "buffer_store_dwordx4 v[192:195], v240, s[40:43], s57 offen sc1\n"
    "s_waitcnt lgkmcnt(1)\n"
    "v_add_f32 v132, v132, v148\n"
    "v_add_f32 v133, v133, v149\n"
    "v_add_f32 v134, v134, v150\n"
    "v_add_f32 v135, v135, v151\n"
    "v_cvt_pk_bf16_f32 v196, v132, v133\n"
    "v_cvt_pk_bf16_f32 v197, v134, v135\n"
    "s_waitcnt lgkmcnt(0)\n"
    "v_add_f32 v140, v140, v156\n"
    "v_add_f32 v141, v141, v157\n"
    "v_add_f32 v142, v142, v158\n"
    "v_add_f32 v143, v143, v159\n"
    "v_cvt_pk_bf16_f32 v198, v140, v141\n"
    "v_cvt_pk_bf16_f32 v199, v142, v143\n"
    "s_nop 1\n"
    "v_permlane16_swap_b32 v196, v198\n"
    "v_permlane16_swap_b32 v197, v199\n"
    "buffer_store_dwordx4 v[196:199], v240, s[40:43], s58 offen sc1\n"
    "s_endpgm\n"
    ".Lg_slowA:\n"
    "ds_read_b64_tr_b16 v[54:55], v219 offset:38912\n"
    "ds_read_b64_tr_b16 v[56:57], v220 offset:36864\n"
    "ds_read_b64_tr_b16 v[58:59], v220 offset:38912\n"
    "ds_read_b64_tr_b16 v[60:61], v221 offset:36864\n"
    "ds_read_b64_tr_b16 v[62:63], v221 offset:38912\n"
    "ds_read_b64_tr_b16 a[128:129], v218 offset:49152\n"
    "ds_read_b64_tr_b16 a[130:131], v218 offset:51200\n"
    "ds_read_b64_tr_b16 a[132:133], v219 offset:49152\n"
    "ds_read_b64_tr_b16 a[134:135], v219 offset:51200\n"
    "ds_read_b64_tr_b16 a[136:137], v220 offset:49152\n"
    "ds_read_b64_tr_b16 a[138:139], v220 offset:51200\n"
    "ds_read_b64_tr_b16 a[140:141], v221 offset:49152\n"
    "ds_read_b64_tr_b16 a[142:143], v221 offset:51200\n"
    "ds_read_b64_tr_b16 a[144:145], v218 offset:53248\n"
    "ds_read_b64_tr_b16 a[146:147], v218 offset:55296\n"
    "ds_read_b64_tr_b16 a[148:149], v219 offset:53248\n"
    "ds_read_b64_tr_b16 a[150:151], v219 offset:55296\n"
    "ds_read_b64_tr_b16 a[152:153], v220 offset:53248\n"
    "ds_read_b64_tr_b16 a[154:155], v220 offset:55296\n"
    "ds_read_b64_tr_b16 a[156:157], v221 offset:53248\n"
    "ds_read_b64_tr_b16 a[158:159], v221 offset:55296\n"
    "ds_read_b64_tr_b16 a[160:161], v214 offset:16384\n"
    "ds_read_b64_tr_b16 a[162:163], v214 offset:18432\n"
    "ds_read_b64_tr_b16 a[164:165], v215 offset:16384\n"
    "ds_read_b64_tr_b16 a[166:167], v215 offset:18432\n"
    "ds_read_b64_tr_b16 a[168:169], v216 offset:16384\n"
    "ds_read_b64_tr_b16 a[170:171], v216 offset:18432\n"
    "ds_read_b64_tr_b16 a[172:173], v217 offset:16384\n"
    "ds_read_b64_tr_b16 a[174:175], v217 offset:18432\n"
    "ds_read_b64_tr_b16 a[176:177], v214 offset:20480\n"
    "ds_read_b64_tr_b16 a[178:179], v214 offset:22528\n"
    "ds_read_b64_tr_b16 a[180:181], v215 offset:20480\n"
    "ds_read_b64_tr_b16 a[182:183], v215 offset:22528\n"
    "ds_read_b64_tr_b16 a[184:185], v216 offset:20480\n"
    "ds_read_b64_tr_b16 a[186:187], v216 offset:22528\n"
    "ds_read_b64_tr_b16 a[188:189], v217 offset:20480\n"
    "ds_read_b64_tr_b16 a[190:191], v217 offset:22528\n"
    "ds_read_b64_tr_b16 v[64:65], v218 offset:16384\n"
    "s_waitcnt lgkmcnt(0)\n"
    "s_waitcnt vmcnt(0)\n"
    "s_barrier\n"
    "s_cmp_lg_u32 s88, 0\n"
    "s_cbranch_scc1 .Lg_noflag_4\n"
    "s_add_u32 s89, s89, 524288\n"
    "buffer_store_dwordx4 v[228:231], v227, s[36:39], s89 offen sc1\n"
    ".Lg_noflag_4:\n"
    ".Lg_spare_join:\n"
    "s_movk_i32 s62, 0x4000\n"
    ".Lg_sp_5:\n"
    "s_load_dwordx2 s[64:65], s[96:97], 0x0 glc\n"
    "s_load_dwordx2 s[66:67], s[96:97], 0x100 glc\n"
    "s_load_dwordx2 s[68:69], s[96:97], 0x200 glc\n"
    "s_load_dwordx2 s[70:71], s[96:97], 0x300 glc\n"
    "s_load_dwordx2 s[72:73], s[96:97], 0x400 glc\n"
    "s_load_dwordx2 s[74:75], s[96:97], 0x500 glc\n"
    "s_load_dwordx2 s[76:77], s[96:97], 0x600 glc\n"
    "s_waitcnt lgkmcnt(0)\n"
    "s_xor_b64 s[64:65], s[64:65], s[44:45]\n"
    "s_xor_b64 s[66:67], s[66:67], s[44:45]\n"
    "s_xor_b64 s[68:69], s[68:69], s[44:45]\n"
    "s_xor_b64 s[70:71], s[70:71], s[44:45]\n"
    "s_xor_b64 s[72:73], s[72:73], s[44:45]\n"
    "s_xor_b64 s[74:75], s[74:75], s[44:45]\n"
    "s_xor_b64 s[76:77], s[76:77], s[44:45]\n"
    "s_or_b64 s[64:65], s[64:65], s[66:67]\n"
    "s_or_b64 s[64:65], s[64:65], s[68:69]\n"
    "s_or_b64 s[64:65], s[64:65], s[70:71]\n"
    "s_or_b64 s[64:65], s[64:65], s[72:73]\n"
    "s_or_b64 s[64:65], s[64:65], s[74:75]\n"
    "s_or_b64 s[64:65], s[64:65], s[76:77]\n"
    "s_cbranch_scc0 .Lg_okA\n"
    "s_sleep 2\n"
    "s_sub_u32 s62, s62, 1\n"
    "s_cbranch_scc0 .Lg_sp_5\n"
    "v_min_u32 v234, 6, v232\n"
    "v_lshlrev_b32 v234, 8, v234\n"
    "s_add_u32 s61, s94, 0\n"
    "s_mov_b32 s62, 0x40000\n"
    ".Lg_poll_6:\n"
    "buffer_load_dwordx2 v[236:237], v234, s[36:39], s61 offen sc1\n"
    "s_waitcnt vmcnt(0)\n"
    "v_cmp_ne_u64 vcc, s[44:45], v[236:237]\n"
    "s_cbranch_vccz .Lg_polldone_7\n"
    "s_sleep 2\n"
    "s_sub_u32 s62, s62, 1\n"
    "s_cbranch_scc0 .Lg_poll_6\n"
    ".Lg_polldone_7:\n"
    ".Lg_okA:\n"
    "buffer_load_dwordx4 a[0:3], v246, s[36:39], s8 offen offset:0 sc1\n"
    "buffer_load_dwordx4 a[4:7], v246, s[36:39], s8 offen offset:2048 sc1\n"
    "buffer_load_dwordx4 a[8:11], v247, s[36:39], s8 offen offset:0 sc1\n"
    "buffer_load_dwordx4 a[12:15], v247, s[36:39], s8 offen offset:2048 sc1\n"
    "buffer_load_dwordx4 a[16:19], v246, s[36:39], s9 offen offset:0 sc1\n"
    "buffer_load_dwordx4 a[20:23], v246, s[36:39], s9 offen offset:2048 sc1\n"
    "buffer_load_dwordx4 a[24:27], v247, s[36:39], s9 offen offset:0 sc1\n"
    "buffer_load_dwordx4 a[28:31], v247, s[36:39], s9 offen offset:2048 sc1\n"
    "s_branch .Lg_joinA\n"
    ".Lg_slowB:\n"
    "s_movk_i32 s62, 0x4000\n"
    ".Lg_sp_8:\n"
    "s_load_dwordx2 s[64:65], s[96:97], 0x80000 glc\n"
    "s_load_dwordx2 s[66:67], s[96:97], 0x80100 glc\n"
    "s_load_dwordx2 s[68:69], s[96:97], 0x80200 glc\n"
    "s_load_dwordx2 s[70:71], s[96:97], 0x80300 glc\n"
    "s_load_dwordx2 s[72:73], s[96:97], 0x80400 glc\n"
    "s_load_dwordx2 s[74:75], s[96:97], 0x80500 glc\n"
    "s_load_dwordx2 s[76:77], s[96:97], 0x80600 glc\n"
    "s_waitcnt lgkmcnt(0)\n"
    "s_xor_b64 s[64:65], s[64:65], s[44:45]\n"
    "s_xor_b64 s[66:67], s[66:67], s[44:45]\n"
    "s_xor_b64 s[68:69], s[68:69], s[44:45]\n"
    "s_xor_b64 s[70:71], s[70:71], s[44:45]\n"
    "s_xor_b64 s[72:73], s[72:73], s[44:45]\n"
    "s_xor_b64 s[74:75], s[74:75], s[44:45]\n"
    "s_xor_b64 s[76:77], s[76:77], s[44:45]\n"
    "s_or_b64 s[64:65], s[64:65], s[66:67]\n"
    "s_or_b64 s[64:65], s[64:65], s[68:69]\n"
    "s_or_b64 s[64:65], s[64:65], s[70:71]\n"
    "s_or_b64 s[64:65], s[64:65], s[72:73]\n"
    "s_or_b64 s[64:65], s[64:65], s[74:75]\n"
    "s_or_b64 s[64:65], s[64:65], s[76:77]\n"
    "s_cbranch_scc0 .Lg_okB\n"
    "s_sleep 2\n"
    "s_sub_u32 s62, s62, 1\n"
    "s_cbranch_scc0 .Lg_sp_8\n"
    "v_min_u32 v234, 6, v232\n"
    "v_lshlrev_b32 v234, 8, v234\n"
    "s_add_u32 s61, s94, 524288\n"
    "s_mov_b32 s62, 0x40000\n"
    ".Lg_poll_9:\n"
    "buffer_load_dwordx2 v[236:237], v234, s[36:39], s61 offen sc1\n"
    "s_waitcnt vmcnt(0)\n"
    "v_cmp_ne_u64 vcc, s[44:45], v[236:237]\n"
    "s_cbranch_vccz .Lg_polldone_10\n"
    "s_sleep 2\n"
    "s_sub_u32 s62, s62, 1\n"
    "s_cbranch_scc0 .Lg_poll_9\n"
    ".Lg_polldone_10:\n"
    "s_branch .Lg_okB\n"
    ".Lg_spare:\n"
    "s_lshl2_add_u32 s54, s49, s59\n"
    "s_xor_b32 s55, s60, 3\n"
    "s_mov_b32 s87, 0\n"
    "s_mov_b32 s36, s18\n"
    "s_and_b32 s37, s19, 0xffff\n"
    "s_mov_b32 s40, s16\n"
    "s_and_b32 s41, s17, 0xffff\n"
    "s_mov_b32 s34, 1835008\n"
    "s_mov_b32 s35, 0x20000\n"
    "s_mov_b32 s38, 33554432\n"
    "s_mov_b32 s39, 0x20000\n"
    "s_mov_b32 s42, 2097152\n"
    "s_mov_b32 s43, 0x20000\n"
    "v_lshlrev_b32 v7, 1, v6\n"
    "v_xor_b32 v7, v5, v7\n"
    "s_and_b32 s92, s51, 1\n"
    "s_lshr_b32 s93, s51, 1\n"
    "s_lshl_b32 s61, s93, 17\n"
    "s_lshl_b32 s62, s92, 15\n"
    "s_add_u32 s61, s61, s62\n"
    "v_lshl_add_u32 v8, v4, 10, s61\n"
    "v_lshl_add_u32 v198, v7, 4, v8\n"
    "v_add_u32 v199, 7168, v198\n"
    "v_add_u32 v200, 14336, v198\n"
    "v_add_u32 v201, 21504, v198\n"
    "s_lshl3_add_u32 s61, s54, s55\n"
    "s_lshl4_add_u32 s61, s61, s92\n"
    "s_lshl_b32 s94, s61, 7\n"
    "s_add_u32 s94, s94, 8388608\n"
    "s_add_u32 s96, s18, s94\n"
    "s_addc_u32 s97, s19, 0\n"
    "s_mul_i32 s61, s93, 524288\n"
    "s_load_dword s95, s[96:97], s61 offset:0x0 glc\n"
    "s_load_dword s95, s[96:97], s61 offset:0x100 glc\n"
    "s_load_dword s95, s[96:97], s61 offset:0x200 glc\n"
    "s_load_dword s95, s[96:97], s61 offset:0x300 glc\n"
    "s_load_dword s95, s[96:97], s61 offset:0x400 glc\n"
    "s_load_dword s95, s[96:97], s61 offset:0x500 glc\n"
    "s_load_dword s95, s[96:97], s61 offset:0x600 glc\n"
    "s_lshl1_add_u32 s61, s54, s93\n"
    "s_mul_i32 s61, s61, 229376\n"
    "s_lshl_b32 s62, s92, 14\n"
    "s_add_u32 s61, s61, s62\n"
    "s_add_u32 s61, s61, 16777216\n"
    "s_lshl_b32 s62, s55, 7\n"
    "s_add_u32 s80, s62, 0\n"
    "s_add_u32 s8, s61, 0\n"
    "s_add_u32 s81, s62, 262144\n"
    "s_add_u32 s9, s61, 32768\n"
    "s_add_u32 s82, s62, 524288\n"
    "s_add_u32 s10, s61, 65536\n"
    "s_add_u32 s83, s62, 786432\n"
    "s_add_u32 s11, s61, 98304\n"
    "s_add_u32 s84, s62, 1048576\n"
    "s_add_u32 s12, s61, 131072\n"
    "s_add_u32 s85, s62, 1310720\n"
    "s_add_u32 s13, s61, 163840\n"
    "s_add_u32 s86, s62, 1572864\n"
    "s_add_u32 s14, s61, 196608\n"
    "s_lshl1_add_u32 s61, s52, s93\n"
    "s_mul_i32 s90, s61, 458752\n"
    "s_lshl2_add_u32 s61, s53, s92\n"
    "s_lshl_b32 s61, s61, 12\n"
    "s_add_u32 s90, s90, s61\n"
    "s_add_u32 s90, s90, 16777216\n"
    "s_add_u32 s91, s90, 229376\n"
    "s_lshl_b32 s89, s52, 8\n"
    "s_add_u32 s89, s89, s53\n"
    "s_lshl_b32 s89, s89, 7\n"
    "s_add_u32 s89, s89, 8388608\n"
    "s_cmp_lt_u32 s50, 4\n"
    "s_cselect_b32 s88, 1, s51\n"
    "v_bfe_u32 v9, v232, 2, 2\n"
    "v_and_b32 v10, 3, v232\n"
    "v_bfe_u32 v11, v232, 3, 2\n"
    "v_lshlrev_b32 v12, 5, v11\n"
    "v_lshl_add_u32 v12, v10, 3, v12\n"
    "v_lshl_add_u32 v12, v9, 7, v12\n"
    "s_lshl_b32 s61, s92, 6\n"
    "v_xor_b32 v12, s61, v12\n"
    "v_and_b32 v13, 1, v6\n"
    "v_lshl_add_u32 v13, v13, 10, v12\n"
    "v_lshrrev_b32 v14, 1, v6\n"
    "v_lshl_add_u32 v202, v14, 12, v13\n"
    "v_xor_b32 v203, 32, v202\n"
    "v_add_u32 v204, 65536, v202\n"
    "v_add_u32 v205, 65536, v203\n"
    "v_add_u32 v206, 131072, v202\n"
    "v_add_u32 v207, 131072, v203\n"
    "s_lshl_b32 s61, s92, 13\n"
    "v_lshl_add_u32 v13, v6, 9, v12\n"
    "v_add_u32 v214, s61, v13\n"
    "v_xor_b32 v215, 32, v214\n"
    "v_xor_b32 v216, 64, v214\n"
    "v_xor_b32 v217, 96, v214\n"
    "v_add_u32 v218, 65536, v214\n"
    "v_add_u32 v219, 65536, v215\n"
    "v_add_u32 v220, 65536, v216\n"
    "v_add_u32 v221, 65536, v217\n"
    "v_add_u32 v222, 131072, v214\n"
    "v_add_u32 v223, 131072, v215\n"
    "v_add_u32 v224, 131072, v216\n"
    "v_add_u32 v225, 131072, v217\n"
    "v_and_b32 v9, 15, v232\n"
    "v_lshrrev_b32 v10, 1, v9\n"
    "v_xor_b32 v10, v10, v6\n"
    "s_lshl_b32 s61, s93, 13\n"
    "v_lshl_add_u32 v9, v9, 7, s61\n"
    "v_lshl_add_u32 v208, v10, 4, v9\n"
    "v_xor_b32 v209, 64, v208\n"
    "v_add_u32 v210, 65536, v208\n"
    "v_add_u32 v211, 65536, v209\n"
    "v_add_u32 v212, 131072, v208\n"
    "v_add_u32 v213, 131072, v209\n"
    "v_lshlrev_b32 v226, 4, v232\n"
    "v_lshrrev_b32 v9, 2, v232\n"
    "v_and_b32 v10, 48, v226\n"
    "v_lshl_add_u32 v227, v9, 11, v10\n"
    "v_lshlrev_b32 v9, 4, v5\n"
    "v_lshl_add_u32 v246, v4, 8, v9\n"
    "v_add_u32 v247, 4096, v246\n"
    "s_lshl2_add_u32 s6, s48, s51\n"
    "s_mul_i32 s6, s6, 7\n"
    "v_min_u32 v234, 6, v4\n"
    "v_add_u32 v234, s6, v234\n"
    "v_and_b32 v235, 0x3c0, v234\n"
    "v_add_u32 v234, v234, v235\n"
    "v_lshlrev_b32 v234, 10, v234\n"
    "v_lshl_add_u32 v234, v5, 7, v234\n"
    "s_mov_b32 s6, 65536\n"
    "buffer_load_dword v138, v234, s[32:35], s6 offen\n"
    "s_add_u32 m0, s63, 0\n"
    "s_nop 0\n"
    "buffer_load_dwordx4 v198, s[32:35], s80 offen offset:0 lds\n"
    "buffer_load_dwordx4 v199, s[32:35], s80 offen offset:1024 lds\n"
    "buffer_load_dwordx4 v200, s[32:35], s80 offen offset:2048 lds\n"
    "buffer_load_dwordx4 v201, s[32:35], s80 offen offset:3072 lds\n"
    "s_add_u32 m0, s63, 16384\n"
    "s_nop 0\n"
    "buffer_load_dwordx4 v198, s[32:35], s81 offen offset:0 lds\n"
    "buffer_load_dwordx4 v199, s[32:35], s81 offen offset:1024 lds\n"
    "buffer_load_dwordx4 v200, s[32:35], s81 offen offset:2048 lds\n"
    "buffer_load_dwordx4 v201, s[32:35], s81 offen offset:3072 lds\n"
    "s_add_u32 m0, s63, 32768\n"
    "s_nop 0\n"
    "buffer_load_dwordx4 v198, s[32:35], s82 offen offset:0 lds\n"
    "buffer_load_dwordx4 v199, s[32:35], s82 offen offset:1024 lds\n"
    "buffer_load_dwordx4 v200, s[32:35], s82 offen offset:2048 lds\n"
    "buffer_load_dwordx4 v201, s[32:35], s82 offen offset:3072 lds\n"
    "s_add_u32 m0, s63, 49152\n"
    "s_nop 0\n"
    "buffer_load_dwordx4 v198, s[32:35], s83 offen offset:0 lds\n"
    "buffer_load_dwordx4 v199, s[32:35], s83 offen offset:1024 lds\n"
    "buffer_load_dwordx4 v200, s[32:35], s83 offen offset:2048 lds\n"
    "buffer_load_dwordx4 v201, s[32:35], s83 offen offset:3072 lds\n"
    "s_add_u32 m0, s63, 65536\n"
    "s_nop 0\n"
    "buffer_load_dwordx4 v198, s[32:35], s84 offen offset:0 lds\n"
    "buffer_load_dwordx4 v199, s[32:35], s84 offen offset:1024 lds\n"
    "buffer_load_dwordx4 v200, s[32:35], s84 offen offset:2048 lds\n"
    "buffer_load_dwordx4 v201, s[32:35], s84 offen offset:3072 lds\n"
    "s_add_u32 m0, s63, 81920\n"
    "s_nop 0\n"
    "buffer_load_dwordx4 v198, s[32:35], s85 offen offset:0 lds\n"
    "buffer_load_dwordx4 v199, s[32:35], s85 offen offset:1024 lds\n"
    "buffer_load_dwordx4 v200, s[32:35], s85 offen offset:2048 lds\n"
    "buffer_load_dwordx4 v201, s[32:35], s85 offen offset:3072 lds\n"
    "s_add_u32 m0, s63, 98304\n"
    "s_nop 0\n"
    "buffer_load_dwordx4 v198, s[32:35], s86 offen offset:0 lds\n"
    "buffer_load_dwordx4 v199, s[32:35], s86 offen offset:1024 lds\n"
    "buffer_load_dwordx4 v200, s[32:35], s86 offen offset:2048 lds\n"
    "buffer_load_dwordx4 v201, s[32:35], s86 offen offset:3072 lds\n"
    "s_add_u32 s99, s80, 65536\n"
    "s_add_u32 m0, s63, 114688\n"
    "s_nop 0\n"
    "buffer_load_dwordx4 v198, s[32:35], s99 offen offset:0 lds\n"
    "buffer_load_dwordx4 v199, s[32:35], s99 offen offset:1024 lds\n"
    "buffer_load_dwordx4 v200, s[32:35], s99 offen offset:2048 lds\n"
    "buffer_load_dwordx4 v201, s[32:35], s99 offen offset:3072 lds\n"
    "s_waitcnt lgkmcnt(0)\n"
    "s_mul_i32 s44, s46, 0x4c957f2d\n"
    "s_mul_hi_u32 s45, s46, 0x4c957f2d\n"
    "s_mul_i32 s57, s46, 0x5851f42d\n"
    "s_add_u32 s45, s45, s57\n"
    "s_mul_i32 s57, s47, 0x4c957f2d\n"
    "s_add_u32 s45, s45, s57\n"
    "s_add_u32 s44, s44, 0xf767814f\n"
    "s_addc_u32 s45, s45, 0x14057b7e\n"
    "v_mov_b32 v228, s44\n"
    "v_mov_b32 v229, s45\n"
    "v_mov_b32 v230, s44\n"
    "v_mov_b32 v231, s45\n"
    "s_waitcnt vmcnt(0)\n"
    "s_barrier\n"
    "s_cmp_lg_u32 s51, 0\n"
    "s_cbranch_scc1 .Lg_nohello_11\n"
    "s_lshl_b32 s57, s48, 7\n"
    "s_add_u32 s57, s57, 10485760\n"
    "s_mov_b64 exec, 15\n"
    "buffer_store_dwordx4 v[228:231], v226, s[36:39], s57 offen sc1\n"
    "s_mov_b64 exec, -1\n"
    ".Lg_nohello_11:\n"
    "ds_read_b64_tr_b16 a[192:193], v214 offset:0\n"
    "ds_read_b64_tr_b16 a[194:195], v214 offset:2048\n"
    "ds_read_b64_tr_b16 a[196:197], v215 offset:0\n"
    "ds_read_b64_tr_b16 a[198:199], v215 offset:2048\n"
    "ds_read_b64_tr_b16 a[200:201], v216 offset:0\n"
    "ds_read_b64_tr_b16 a[202:203], v216 offset:2048\n"
    "ds_read_b64_tr_b16 a[204:205], v217 offset:0\n"
    "ds_read_b64_tr_b16 a[206:207], v217 offset:2048\n"
    "ds_read_b64_tr_b16 a[208:209], v214 offset:4096\n"
    "ds_read_b64_tr_b16 a[210:211], v214 offset:6144\n"
    "ds_read_b64_tr_b16 a[212:213], v215 offset:4096\n"
    "ds_read_b64_tr_b16 a[214:215], v215 offset:6144\n"
    "ds_read_b64_tr_b16 a[216:217], v216 offset:4096\n"
    "ds_read_b64_tr_b16 a[218:219], v216 offset:6144\n"
    "ds_read_b64_tr_b16 a[220:221], v217 offset:4096\n"
    "ds_read_b64_tr_b16 a[222:223], v217 offset:6144\n"
    "ds_read_b64_tr_b16 a[224:225], v214 offset:16384\n"
    "ds_read_b64_tr_b16 a[226:227], v214 offset:18432\n"
    "ds_read_b64_tr_b16 a[228:229], v215 offset:16384\n"
    "ds_read_b64_tr_b16 a[230:231], v215 offset:18432\n"
    "ds_read_b64_tr_b16 a[232:233], v216 offset:16384\n"
    "ds_read_b64_tr_b16 a[234:235], v216 offset:18432\n"
    "ds_read_b64_tr_b16 a[236:237], v217 offset:16384\n"
    "ds_read_b64_tr_b16 a[238:239], v217 offset:18432\n"
    "ds_read_b64_tr_b16 a[240:241], v214 offset:20480\n"
    "ds_read_b64_tr_b16 a[242:243], v214 offset:22528\n"
    "ds_read_b64_tr_b16 a[244:245], v215 offset:20480\n"
    "ds_read_b64_tr_b16 a[246:247], v215 offset:22528\n"
    "ds_read_b64_tr_b16 a[248:249], v216 offset:20480\n"
    "ds_read_b64_tr_b16 a[250:251], v216 offset:22528\n"
    "ds_read_b64_tr_b16 a[252:253], v217 offset:20480\n"
    "ds_read_b64_tr_b16 a[254:255], v217 offset:22528\n"
    "ds_read_b64_tr_b16 a[64:65], v214 offset:32768\n"
    "ds_read_b64_tr_b16 a[66:67], v214 offset:34816\n"
    "ds_read_b64_tr_b16 a[68:69], v215 offset:32768\n"
    "ds_read_b64_tr_b16 a[70:71], v215 offset:34816\n"
    "ds_read_b64_tr_b16 a[72:73], v216 offset:32768\n"
    "ds_read_b64_tr_b16 a[74:75], v216 offset:34816\n"
    "ds_read_b64_tr_b16 a[76:77], v217 offset:32768\n"
    "ds_read_b64_tr_b16 a[78:79], v217 offset:34816\n"
    "ds_read_b64_tr_b16 a[80:81], v214 offset:36864\n"
    "ds_read_b64_tr_b16 a[82:83], v214 offset:38912\n"
    "ds_read_b64_tr_b16 a[84:85], v215 offset:36864\n"
    "ds_read_b64_tr_b16 a[86:87], v215 offset:38912\n"
    "ds_read_b64_tr_b16 a[88:89], v216 offset:36864\n"
    "ds_read_b64_tr_b16 a[90:91], v216 offset:38912\n"
    "ds_read_b64_tr_b16 a[92:93], v217 offset:36864\n"
    "ds_read_b64_tr_b16 a[94:95], v217 offset:38912\n"
    "ds_read_b64_tr_b16 v[0:1], v214 offset:49152\n"
    "ds_read_b64_tr_b16 v[2:3], v214 offset:51200\n"
    "ds_read_b64_tr_b16 v[4:5], v215 offset:49152\n"
    "ds_read_b64_tr_b16 v[6:7], v215 offset:51200\n"
    "ds_read_b64_tr_b16 v[8:9], v216 offset:49152\n"
    "ds_read_b64_tr_b16 v[10:11], v216 offset:51200\n"
    "ds_read_b64_tr_b16 v[12:13], v217 offset:49152\n"
    "ds_read_b64_tr_b16 v[14:15], v217 offset:51200\n"
    "ds_read_b64_tr_b16 v[16:17], v214 offset:53248\n"
    "ds_read_b64_tr_b16 v[18:19], v214 offset:55296\n"
    "ds_read_b64_tr_b16 v[20:21], v215 offset:53248\n"
    "ds_read_b64_tr_b16 v[22:23], v215 offset:55296\n"
    "ds_read_b64_tr_b16 v[24:25], v216 offset:53248\n"
    "ds_read_b64_tr_b16 v[26:27], v216 offset:55296\n"
    "ds_read_b64_tr_b16 v[28:29], v217 offset:53248\n"
    "ds_read_b64_tr_b16 v[30:31], v217 offset:55296\n"
    "ds_read_b64_tr_b16 a[96:97], v218 offset:0\n"
    "ds_read_b64_tr_b16 a[98:99], v218 offset:2048\n"
    "ds_read_b64_tr_b16 a[100:101], v219 offset:0\n"
    "ds_read_b64_tr_b16 a[102:103], v219 offset:2048\n"
    "ds_read_b64_tr_b16 a[104:105], v220 offset:0\n"
    "ds_read_b64_tr_b16 a[106:107], v220 offset:2048\n"
    "ds_read_b64_tr_b16 a[108:109], v221 offset:0\n"
    "ds_read_b64_tr_b16 a[110:111], v221 offset:2048\n"
    "ds_read_b64_tr_b16 a[112:113], v218 offset:4096\n"
    "ds_read_b64_tr_b16 a[114:115], v218 offset:6144\n"
    "ds_read_b64_tr_b16 a[116:117], v219 offset:4096\n"
    "ds_read_b64_tr_b16 a[118:119], v219 offset:6144\n"
    "ds_read_b64_tr_b16 a[120:121], v220 offset:4096\n"
    "ds_read_b64_tr_b16 a[122:123], v220 offset:6144\n"
    "ds_read_b64_tr_b16 a[124:125], v221 offset:4096\n"
    "ds_read_b64_tr_b16 a[126:127], v221 offset:6144\n"
    "ds_read_b64_tr_b16 v[32:33], v218 offset:16384\n"
    "ds_read_b64_tr_b16 v[34:35], v218 offset:18432\n"
    "ds_read_b64_tr_b16 v[36:37], v219 offset:16384\n"
    "ds_read_b64_tr_b16 v[38:39], v219 offset:18432\n"
    "ds_read_b64_tr_b16 v[40:41], v220 offset:16384\n"
    "ds_read_b64_tr_b16 v[42:43], v220 offset:18432\n"
    "ds_read_b64_tr_b16 v[44:45], v221 offset:16384\n"
    "ds_read_b64_tr_b16 v[46:47], v221 offset:18432\n"
    "ds_read_b64_tr_b16 v[48:49], v218 offset:20480\n"
    "ds_read_b64_tr_b16 v[50:51], v218 offset:22528\n"
    "ds_read_b64_tr_b16 v[52:53], v219 offset:20480\n"
    "ds_read_b64_tr_b16 v[54:55], v219 offset:22528\n"
    "ds_read_b64_tr_b16 v[56:57], v220 offset:20480\n"
    "ds_read_b64_tr_b16 v[58:59], v220 offset:22528\n"
    "ds_read_b64_tr_b16 v[60:61], v221 offset:20480\n"
    "ds_read_b64_tr_b16 v[62:63], v221 offset:22528\n"
    "ds_read_b64_tr_b16 a[128:129], v218 offset:32768\n"
    "ds_read_b64_tr_b16 a[130:131], v218 offset:34816\n"
    "ds_read_b64_tr_b16 a[132:133], v219 offset:32768\n"
    "ds_read_b64_tr_b16 a[134:135], v219 offset:34816\n"
    "ds_read_b64_tr_b16 a[136:137], v220 offset:32768\n"
    "ds_read_b64_tr_b16 a[138:139], v220 offset:34816\n"
    "ds_read_b64_tr_b16 a[140:141], v221 offset:32768\n"
    "ds_read_b64_tr_b16 a[142:143], v221 offset:34816\n"
    "ds_read_b64_tr_b16 a[144:145], v218 offset:36864\n"
    "ds_read_b64_tr_b16 a[146:147], v218 offset:38912\n"
    "ds_read_b64_tr_b16 a[148:149], v219 offset:36864\n"
    "ds_read_b64_tr_b16 a[150:151], v219 offset:38912\n"
    "ds_read_b64_tr_b16 a[152:153], v220 offset:36864\n"
    "ds_read_b64_tr_b16 a[154:155], v220 offset:38912\n"
    "ds_read_b64_tr_b16 a[156:157], v221 offset:36864\n"
    "ds_read_b64_tr_b16 a[158:159], v221 offset:38912\n"
    "ds_read_b64_tr_b16 a[160:161], v218 offset:49152\n"
    "ds_read_b64_tr_b16 a[162:163], v218 offset:51200\n"
    "ds_read_b64_tr_b16 a[164:165], v219 offset:49152\n"
    "ds_read_b64_tr_b16 a[166:167], v219 offset:51200\n"
    "ds_read_b64_tr_b16 a[168:169], v220 offset:49152\n"
    "ds_read_b64_tr_b16 a[170:171], v220 offset:51200\n"
    "ds_read_b64_tr_b16 a[172:173], v221 offset:49152\n"
    "ds_read_b64_tr_b16 a[174:175], v221 offset:51200\n"
    "ds_read_b64_tr_b16 a[176:177], v218 offset:53248\n"
    "ds_read_b64_tr_b16 a[178:179], v218 offset:55296\n"
    "ds_read_b64_tr_b16 a[180:181], v219 offset:53248\n"
    "ds_read_b64_tr_b16 a[182:183], v219 offset:55296\n"
    "ds_read_b64_tr_b16 a[184:185], v220 offset:53248\n"
    "ds_read_b64_tr_b16 a[186:187], v220 offset:55296\n"
    "ds_read_b64_tr_b16 a[188:189], v221 offset:53248\n"
    "ds_read_b64_tr_b16 a[190:191], v221 offset:55296\n"
    "s_waitcnt lgkmcnt(0)\n"
    "s_barrier\n"
    "s_and_b32 s60, s48, 0xf9\n"
    "s_or_b32 s60, s60, s51\n"
    "v_lshrrev_b32 v234, 1, v232\n"
    "v_and_b32 v235, 1, v232\n"
    "v_lshlrev_b32 v234, 10, v234\n"
    "v_lshl_add_u32 v234, v235, 7, v234\n"
    "s_lshl_b32 s59, s48, 7\n"
    "s_add_u32 s59, s59, 10485760\n"
    "s_cmp_lg_u32 s60, 0\n"
    "s_cbranch_scc1 .Lg_noupd0\n"
    "buffer_load_dwordx2 v[128:129], v234, s[36:39], s59 offen sc1\n"
    ".Lg_noupd0:\n"
    "s_add_u32 s98, s81, 65536\n"
    "s_add_u32 m0, s63, 81920\n"
    "s_nop 0\n"
    "buffer_load_dwordx4 v198, s[32:35], s98 offen offset:0 lds\n"
    "buffer_load_dwordx4 v199, s[32:35], s98 offen offset:1024 lds\n"
    "buffer_load_dwordx4 v200, s[32:35], s98 offen offset:2048 lds\n"
    "buffer_load_dwordx4 v201, s[32:35], s98 offen offset:3072 lds\n"
    "s_add_u32 s99, s82, 65536\n"
    "s_add_u32 m0, s63, 65536\n"
    "s_nop 0\n"
    "buffer_load_dwordx4 v198, s[32:35], s99 offen offset:0 lds\n"
    "buffer_load_dwordx4 v199, s[32:35], s99 offen offset:1024 lds\n"
    "buffer_load_dwordx4 v200, s[32:35], s99 offen offset:2048 lds\n"
    "buffer_load_dwordx4 v201, s[32:35], s99 offen offset:3072 lds\n"
    "s_add_u32 s98, s83, 65536\n"
    "s_add_u32 m0, s63, 32768\n"
    "s_nop 0\n"
    "buffer_load_dwordx4 v198, s[32:35], s98 offen offset:0 lds\n"
    "buffer_load_dwordx4 v199, s[32:35], s98 offen offset:1024 lds\n"
    "buffer_load_dwordx4 v200, s[32:35], s98 offen offset:2048 lds\n"
    "buffer_load_dwordx4 v201, s[32:35], s98 offen offset:3072 lds\n"
    "s_add_u32 s99, s84, 65536\n"
    "s_add_u32 m0, s63, 49152\n"
    "s_nop 0\n"
    "buffer_load_dwordx4 v198, s[32:35], s99 offen offset:0 lds\n"
    "buffer_load_dwordx4 v199, s[32:35], s99 offen offset:1024 lds\n"
    "buffer_load_dwordx4 v200, s[32:35], s99 offen offset:2048 lds\n"
    "buffer_load_dwordx4 v201, s[32:35], s99 offen offset:3072 lds\n"
    "s_add_u32 s98, s85, 65536\n"
    "s_add_u32 m0, s63, 131072\n"
    "s_nop 0\n"
    "buffer_load_dwordx4 v198, s[32:35], s98 offen offset:0 lds\n"
    "buffer_load_dwordx4 v199, s[32:35], s98 offen offset:1024 lds\n"
    "buffer_load_dwordx4 v200, s[32:35], s98 offen offset:2048 lds\n"
    "buffer_load_dwordx4 v201, s[32:35], s98 offen offset:3072 lds\n"
    "s_waitcnt vmcnt(0)\n"
    "s_barrier\n"
    "ds_read_b64_tr_b16 v[64:65], v218 offset:16384\n"
    "s_waitcnt lgkmcnt(0)\n"
    "s_cmp_lg_u32 s60, 0\n"
    "s_cbranch_scc1 .Lg_spare_join\n"
    "v_cmp_ne_u64 vcc, s[44:45], v[128:129]\n"
    "s_cbranch_vccz .Lg_upd\n"
    "s_mov_b32 s62, 0x40000\n"
    ".Lg_poll_12:\n"
    "buffer_load_dwordx2 v[128:129], v234, s[36:39], s59 offen sc1\n"
    "s_waitcnt vmcnt(0)\n"
    "v_cmp_ne_u64 vcc, s[44:45], v[128:129]\n"
    "s_cbranch_vccz .Lg_polldone_13\n"
    "s_sleep 2\n"
    "s_sub_u32 s62, s62, 1\n"
    "s_cbranch_scc0 .Lg_poll_12\n"
    ".Lg_polldone_13:\n"
    ".Lg_upd:\n"
    "s_add_u32 s57, s15, 10518528\n"
    "s_mov_b64 exec, 1\n"
    "buffer_store_dwordx2 v[228:229], off, s[36:39], s57 sc1\n"
    "s_mov_b64 exec, -1\n"
    ".Lg_last:\n"
    "s_branch .Lg_spare_join\n"
    : "+v"(tid)
    : "s"(__builtin_amdgcn_kernarg_segment_ptr()), "s"(blockIdx.x), "s"((unsigned)(size_t)(__attribute__((address_space(3))) char*)smem)
    : "v1", "v2", "v3", "v4", "v5", "v6", "v7", "v8", "v9", "v10", "v11", "v12", "v13", "v14", "v15", "v16", "v17", "v18", "v19", "v20", "v21", "v22", "v23", "v24", "v25", "v26", "v27", "v28", "v29", "v30", "v31", "v32", "v33", "v34", "v35", "v36", "v37", "v38", "v39", "v40", "v41", "v42", "v43", "v44", "v45", "v46", "v47", "v48", "v49", "v50", "v51", "v52", "v53", "v54", "v55", "v56", "v57", "v58", "v59", "v60", "v61", "v62", "v63", "v64", "v65", "v66", "v67", "v68", "v69", "v70", "v71", "v72", "v73", "v74", "v75", "v76", "v77", "v78", "v79", "v80", "v81", "v82", "v83", "v84", "v85", "v86", "v87", "v88", "v89", "v90", "v91", "v92", "v93", "v94", "v95", "v96", "v97", "v98", "v99", "v100", "v101", "v102", "v103", "v104", "v105", "v106", "v107", "v108", "v109", "v110", "v111", "v112", "v113", "v114", "v115", "v116", "v117", "v118", "v119", "v120", "v121", "v122", "v123", "v124", "v125", "v126", "v127", "v128", "v129", "v130", "v131", "v132", "v133", "v134", "v135", "v136", "v137", "v138", "v139", "v140", "v141", "v142", "v143", "v144", "v145", "v146", "v147", "v148", "v149", "v150", "v151", "v152", "v153", "v154", "v155", "v156", "v157", "v158", "v159", "v160", "v161", "v162", "v163", "v164", "v165", "v166", "v167", "v168", "v169", "v170", "v171", "v172", "v173", "v174", "v175", "v176", "v177", "v178", "v179", "v180", "v181", "v182", "v183", "v184", "v185", "v186", "v187", "v188", "v189", "v190", "v191", "v192", "v193", "v194", "v195", "v196", "v197", "v198", "v199", "v200", "v201", "v202", "v203", "v204", "v205", "v206", "v207", "v208", "v209", "v210", "v211", "v212", "v213", "v214", "v215", "v216", "v217", "v218", "v219", "v220", "v221", "v222", "v223", "v224", "v225", "v226", "v227", "v228", "v229", "v230", "v231", "v232", "v233", "v234", "v235", "v236", "v237", "v238", "v239", "v240", "v241", "v242", "v243", "v244", "v245", "v246", "v247", "v248", "v249", "v250", "v251", "v252", "v253", "v254", "v255", "a0", "a1", "a2", "a3", "a4", "a5", "a6", "a7", "a8", "a9", "a10", "a11", "a12", "a13", "a14", "a15", "a16", "a17", "a18", "a19", "a20", "a21", "a22", "a23", "a24", "a25", "a26", "a27", "a28", "a29", "a30", "a31", "a32", "a33", "a34", "a35", "a36", "a37", "a38", "a39", "a40", "a41", "a42", "a43", "a44", "a45", "a46", "a47", "a48", "a49", "a50", "a51", "a52", "a53", "a54", "a55", "a56", "a57", "a58", "a59", "a60", "a61", "a62", "a63", "a64", "a65", "a66", "a67", "a68", "a69", "a70", "a71", "a72", "a73", "a74", "a75", "a76", "a77", "a78", "a79", "a80", "a81", "a82", "a83", "a84", "a85", "a86", "a87", "a88", "a89", "a90", "a91", "a92", "a93", "a94", "a95", "a96", "a97", "a98", "a99", "a100", "a101", "a102", "a103", "a104", "a105", "a106", "a107", "a108", "a109", "a110", "a111", "a112", "a113", "a114", "a115", "a116", "a117", "a118", "a119", "a120", "a121", "a122", "a123", "a124", "a125", "a126", "a127", "a128", "a129", "a130", "a131", "a132", "a133", "a134", "a135", "a136", "a137", "a138", "a139", "a140", "a141", "a142", "a143", "a144", "a145", "a146", "a147", "a148", "a149", "a150", "a151", "a152", "a153", "a154", "a155", "a156", "a157", "a158", "a159", "a160", "a161", "a162", "a163", "a164", "a165", "a166", "a167", "a168", "a169", "a170", "a171", "a172", "a173", "a174", "a175", "a176", "a177", "a178", "a179", "a180", "a181", "a182", "a183", "a184", "a185", "a186", "a187", "a188", "a189", "a190", "a191", "a192", "a193", "a194", "a195", "a196", "a197", "a198", "a199", "a200", "a201", "a202", "a203", "a204", "a205", "a206", "a207", "a208", "a209", "a210", "a211", "a212", "a213", "a214", "a215", "a216", "a217", "a218", "a219", "a220", "a221", "a222", "a223", "a224", "a225", "a226", "a227", "a228", "a229", "a230", "a231", "a232", "a233", "a234", "a235", "a236", "a237", "a238", "a239", "a240", "a241", "a242", "a243", "a244", "a245", "a246", "a247", "a248", "a249", "a250", "a251", "a252", "a253", "a254", "a255", "s4", "s5", "s6", "s7", "s8", "s9", "s10", "s11", "s12", "s13", "s14", "s15", "s16", "s17", "s18", "s19", "s20", "s21", "s22", "s23", "s24", "s25", "s26", "s27", "s28", "s29", "s30", "s31", "s33", "s34", "s35", "s36", "s37", "s38", "s39", "s40", "s41", "s42", "s43", "s44", "s45", "s46", "s47", "s48", "s49", "s50", "s51", "s52", "s53", "s54", "s55", "s56", "s57", "s58", "s59", "s60", "s61", "s62", "s63", "s64", "s65", "s66", "s67", "s68", "s69", "s70", "s71", "s72", "s73", "s74", "s75", "s76", "s77", "s78", "s79", "s80", "s81", "s82", "s83", "s84", "s85", "s86", "s87", "s88", "s89", "s90", "s91", "s92", "s93", "s94", "s95", "s96", "s97", "s98", "s99", "vcc", "scc", "memory");
}

extern "C" void kernel_launch(void* const* d_in, const int* in_sizes, int n_in, void* d_out, int out_size, void* d_ws, size_t ws_size,
                              hipStream_t stream) {
  glu_fused<<<256, 256, 0, stream>>>(d_in[0], d_in[1], d_in[2], d_in[3], d_out, d_ws);
}
```
